```python
import torch

def _softmax(x, axis=-1):
    return torch.softmax(x, dim=axis)
def _gelu(x):
    return torch.nn.functional.gelu(x, approximate="tanh")
def _silu(x):
    return torch.nn.functional.silu(x)
def _sigmoid(x):
    return torch.sigmoid(x)
def _layernorm(x, axis=-1, eps=1e-5):
    m = x.mean(dim=axis, keepdim=True)
    v = x.var(dim=axis, keepdim=True, unbiased=False)
    return (x - m) / torch.sqrt(v + eps)
def _scatter_add(tgt, idx, src, axis=0):
    return tgt.clone().index_add_(axis, idx, src)
def _segment_sum(src, seg, n):
    out = torch.zeros((n,) + src.shape[1:], dtype=src.dtype)
    return out.index_add_(0, seg, src)
def _pad(x, pw):
    flat = tuple(v for pair in reversed(pw) for v in pair)
    return torch.nn.functional.pad(x, flat)

kernel_name = 'sequential_preLN__linear_kernel_hedgehog__glu_gated_geglu__prompt'

def setup_inputs():

    _w = (lambda a: (lambda t: t.to(torch.bfloat16) if t.is_floating_point() else t)(torch.as_tensor(a)))
    def _A_setup():
        B, S, D, H = 1, 2048, 640, 32
        Dh = D // H
        return {
            "x": _w((lambda *s: torch.randn(*s, dtype=torch.bfloat16) * 0.05)(B, S, D)),
            "wqkv": _w((lambda *s: torch.randn(*s, dtype=torch.bfloat16) * 0.2)(D, 3 * D)),
            "w_phi": _w((lambda *s: torch.randn(*s, dtype=torch.bfloat16) * 0.2)(H, Dh, Dh) * (Dh ** -0.5)),
            "wo": _w((lambda *s: torch.randn(*s, dtype=torch.bfloat16) * 0.05)(D, D)),
        }
    _A_in = _A_setup()

    def _B_setup():
        B, S, Dm, Df = 1, 2048, 640, 1728
        return {
            "x": _w((lambda *s: torch.randn(*s, dtype=torch.bfloat16) * 0.05)(B, S, Dm)),
            "w_gate": _w((lambda *s: torch.randn(*s, dtype=torch.bfloat16) * 0.4)(Dm, Df) * (Dm ** -0.5)),
            "w_up": _w((lambda *s: torch.randn(*s, dtype=torch.bfloat16) * 0.4)(Dm, Df) * (Dm ** -0.5)),
            "w_down": _w((lambda *s: torch.randn(*s, dtype=torch.bfloat16) * 0.4)(Df, Dm) * (Df ** -0.5)),
        }
    _B_in = _B_setup()

    return {
        "A_x": _A_in["x"],
        "A_wqkv": _A_in["wqkv"],
        "A_w_phi": _A_in["w_phi"],
        "A_wo": _A_in["wo"],
        "B_x": _B_in["x"],
        "B_w_gate": _B_in["w_gate"],
        "B_w_up": _B_in["w_up"],
        "B_w_down": _B_in["w_down"],
    }

def reference(A_x, A_wqkv, A_w_phi, A_wo, B_x, B_w_gate, B_w_up, B_w_down):
    def _A_ref(x, wqkv, w_phi, wo):
        B, S, D = x.shape
        H = 32; Dh = D // H
        qkv = (lambda x, *d: x.permute(*d))((x @ wqkv).reshape(B, S, 3, H, Dh), 2, 0, 3, 1, 4)
        q, k, v = qkv[0], qkv[1], qkv[2]
        pq = torch.einsum("bhsd,hdf->bhsf", q, w_phi)
        pk = torch.einsum("bhsd,hdf->bhsf", k, w_phi)
        phi_q = torch.concatenate([_softmax(pq, axis=-1), _softmax(-pq, axis=-1)], axis=-1)
        phi_k = torch.concatenate([_softmax(pk, axis=-1), _softmax(-pk, axis=-1)], axis=-1)
        kv = torch.einsum("bhsf,bhsd->bhfd", phi_k, v)
        z  = torch.einsum("bhsf->bhf", phi_k)
        num = torch.einsum("bhqf,bhfd->bhqd", phi_q, kv)
        den = torch.einsum("bhqf,bhf->bhq", phi_q, z)[..., None] + 1e-6
        o = num / den
        return (lambda x, *d: x.permute(*d))(o, 0, 2, 1, 3).reshape(B, S, D) @ wo

    def _B_ref(x, w_gate, w_up, w_down):
        g = torch.einsum("bsd,df->bsf", x, w_gate)
        u = torch.einsum("bsd,df->bsf", x, w_up)
        h = _gelu(g) * u
        return torch.einsum("bsf,fd->bsd", h, w_down)

    x = A_x
    h = x + _A_ref(_layernorm(x, axis=-1), A_wqkv, A_w_phi, A_wo)
    h2 = h + _B_ref(_layernorm(h, axis=-1), B_w_gate, B_w_up, B_w_down)
    return h2
```

```cpp
#include <hip/hip_runtime.h>
#include <hip/hip_bf16.h>
typedef unsigned short u16;
constexpr int S=2048, D=640, H=32, DH=20, DF=1728;
__device__ inline float b2f(u16 v){ return __uint_as_float(((unsigned)v)<<16); }
__device__ inline u16 f2b(float f){ __hip_bfloat16 b = __float2bfloat16(f); return *(u16*)&b; }

__global__ void ln_k(const u16* x, const float* xf, float* y){
  int r=blockIdx.x, t=threadIdx.x;
  __shared__ float sh[64];
  float v[10]; float s=0;
  for(int i=0;i<10;i++){ v[i]= x? b2f(x[r*D+t+64*i]) : xf[r*D+t+64*i]; s+=v[i]; }
  for(int o=32;o;o>>=1) s+=__shfl_xor(s,o);
  float m=s/D; float q=0;
  for(int i=0;i<10;i++){ float d=v[i]-m; q+=d*d; }
  for(int o=32;o;o>>=1) q+=__shfl_xor(q,o);
  float rs=1.f/sqrtf(q/D+1e-5f);
  for(int i=0;i<10;i++) y[r*D+t+64*i]=(v[i]-m)*rs;
}
__global__ void gemm_k(const float* A, const u16* W, float* C, int N, int K, const u16* resb, const float* resf, u16* outb){
  __shared__ float As[16][17], Ws[16][17];
  int tx=threadIdx.x&15, ty=threadIdx.x>>4;
  int row=blockIdx.y*16+ty, col=blockIdx.x*16+tx;
  float acc=0;
  for(int k=0;k<K;k+=16){
    As[ty][tx]=A[row*K+k+tx];
    Ws[ty][tx]=b2f(W[(k+ty)*N+col]);
    __syncthreads();
    for(int j=0;j<16;j++) acc+=As[ty][j]*Ws[j][tx];
    __syncthreads();
  }
  if(resb) acc+=b2f(resb[row*N+col]);
  if(resf) acc+=resf[row*N+col];
  if(outb) outb[row*N+col]=f2b(acc);
  if(C) C[row*N+col]= resb? b2f(f2b(acc)) : acc;
}
__global__ void phi_k(const float* qkv, const u16* wphi, float* phi){
  int idx=blockIdx.x*blockDim.x+threadIdx.x;
  int s=idx/64, w=(idx/32)&1, h=idx&31;
  float p[DH];
  for(int f=0;f<DH;f++){ float a=0; for(int d=0;d<DH;d++) a+=qkv[s*1920+w*640+h*DH+d]*b2f(wphi[(h*DH+d)*DH+f]); p[f]=a; }
  float mx=-1e30f, mn=1e30f;
  for(int f=0;f<DH;f++){ mx=fmaxf(mx,p[f]); mn=fminf(mn,p[f]); }
  float s1=0,s2=0;
  for(int f=0;f<DH;f++){ s1+=__expf(p[f]-mx); s2+=__expf(mn-p[f]); }
  float* o=phi+((size_t)(w*S+s)*H+h)*40;
  for(int f=0;f<DH;f++){ o[f]=__expf(p[f]-mx)/s1; o[20+f]=__expf(mn-p[f])/s2; }
}
__global__ void kv_k(const float* qkv, const float* phi, float* kv){
  int h=blockIdx.x, t=threadIdx.x; if(t>=40*21) return;
  int f=t/21, d=t%21; float a=0;
  for(int s=0;s<S;s++){ float pk=phi[((size_t)(S+s)*H+h)*40+f]; float v= d<20? qkv[s*1920+1280+h*DH+d] : 1.f; a+=pk*v; }
  kv[(h*40+f)*21+d]=a;
}
__global__ void o_k(const float* phi, const float* kv, float* o){
  int idx=blockIdx.x*blockDim.x+threadIdx.x; int s=idx/640, c=idx%640, h=c/20, d=c%20;
  float num=0, den=0;
  for(int f=0;f<40;f++){ float pq=phi[((size_t)s*H+h)*40+f]; num+=pq*kv[(h*40+f)*21+d]; den+=pq*kv[(h*40+f)*21+20]; }
  o[idx]=num/(den+1e-6f);
}
__global__ void glu_k(const float* g, const float* u, float* a){
  int idx=blockIdx.x*blockDim.x+threadIdx.x;
  float x=g[idx]; float t=tanhf(0.7978845608028654f*(x+0.044715f*x*x*x));
  a[idx]=0.5f*x*(1.f+t)*u[idx];
}
extern "C" void kernel_launch(void* const* d_in, const int* in_sizes, int n_in, void* d_out, int out_size, void* d_ws, size_t ws_size, hipStream_t stream) {
  const u16* x=(const u16*)d_in[0]; const u16* wqkv=(const u16*)d_in[1]; const u16* wphi=(const u16*)d_in[2]; const u16* wo=(const u16*)d_in[3];
  const u16* wg=(const u16*)d_in[5]; const u16* wu=(const u16*)d_in[6]; const u16* wd=(const u16*)d_in[7];
  float* ws=(float*)d_ws;
  float* xn=ws; float* qkv=xn+S*D; float* phi=qkv+S*1920; float* kv=phi+2*S*1280; float* o=kv+32*40*21+64; float* h=o+S*D; float* hn=h+S*D; float* g=hn+S*D; float* u=g+S*DF; float* a=u+S*DF;
  ln_k<<<S,64,0,stream>>>(x,nullptr,xn);
  gemm_k<<<dim3(1920/16,S/16),256,0,stream>>>(xn,wqkv,qkv,1920,D,nullptr,nullptr,nullptr);
  phi_k<<<S*64/256,256,0,stream>>>(qkv,wphi,phi);
  kv_k<<<32,1024,0,stream>>>(qkv,phi,kv);
  o_k<<<S*640/256,256,0,stream>>>(phi,kv,o);
  gemm_k<<<dim3(640/16,S/16),256,0,stream>>>(o,wo,h,640,D,x,nullptr,nullptr);
  ln_k<<<S,64,0,stream>>>(nullptr,h,hn);
  gemm_k<<<dim3(DF/16,S/16),256,0,stream>>>(hn,wg,g,DF,D,nullptr,nullptr,nullptr);
  gemm_k<<<dim3(DF/16,S/16),256,0,stream>>>(hn,wu,u,DF,D,nullptr,nullptr,nullptr);
  glu_k<<<S*DF/256,256,0,stream>>>(g,u,a);
  gemm_k<<<dim3(640/16,S/16),256,0,stream>>>(a,wd,nullptr,640,DF,nullptr,h,(u16*)d_out);
}
```

```cpp
#include <hip/hip_runtime.h>

typedef unsigned short u16;
typedef unsigned long long u64;
typedef __bf16 bf16x8 __attribute__((ext_vector_type(8)));
typedef __bf16 bf16x2 __attribute__((ext_vector_type(2)));
typedef short s16x4 __attribute__((ext_vector_type(4)));
typedef float f32x4 __attribute__((ext_vector_type(4)));
typedef float f32x2 __attribute__((ext_vector_type(2)));
typedef unsigned u32x4 __attribute__((ext_vector_type(4)));
typedef unsigned u32x2 __attribute__((ext_vector_type(2)));
typedef __amdgpu_buffer_rsrc_t rsrc_t;
#define LAS __attribute__((address_space(3)))
#define GAS __attribute__((address_space(1)))
#define DEVI __device__ __forceinline__

constexpr int S = 2048, D = 640, DF = 1728;
constexpr int NT = 512;

constexpr unsigned WS_EPOCH = 0;
constexpr unsigned WS_FKV = 8192;
constexpr unsigned WS_KVP = 65536;
constexpr unsigned KVP_SZ = 3456;
constexpr unsigned WS_O = 1u << 20;
constexpr unsigned WS_H = WS_O + 128 * 20 * 1024;
constexpr unsigned WS_ST = WS_H + 128 * 20 * 1024;
constexpr unsigned WS_ACT = WS_ST + 10 * 2048 * 8;
constexpr unsigned WS_END = WS_ACT + 128 * 54 * 1024;

struct Params {
  const u16* x; const u16* wqkv; const u16* wphi; const u16* wo;
  const u16* wg; const u16* wu; const u16* wd; u16* out; char* ws;
};

DEVI rsrc_t mk_rsrc(const void* p, unsigned bytes) { return __builtin_amdgcn_make_buffer_rsrc((void*)p, 0, bytes, 0x00020000); }
DEVI u32x4 mk_srd(const void* p, unsigned bytes) {
  const u64 a = (u64)p;
  return (u32x4){(unsigned)a, (unsigned)(a >> 32) & 0xffffu, bytes, 0x00020000u};
}
template <int AUX> DEVI void dma16(u32x4 srd, unsigned lds, unsigned voff, unsigned soff) {
  if constexpr (AUX == 0)
    asm volatile("s_mov_b32 m0, %0\n\ts_nop 0\n\tbuffer_load_dwordx4 %1, %2, %3 offen lds" ::"s"(lds), "v"(voff), "s"(srd), "s"(soff) : "memory");
  else
    asm volatile("s_mov_b32 m0, %0\n\ts_nop 0\n\tbuffer_load_dwordx4 %1, %2, %3 offen sc1 lds" ::"s"(lds), "v"(voff), "s"(srd), "s"(soff) : "memory");
}
template <int AUX = 0> DEVI void dma16m(u32x4 srd, unsigned lds, unsigned voff, unsigned soff, u64 mask) {
  if constexpr (AUX == 0)
    asm volatile("s_mov_b32 m0, %0\n\ts_mov_b64 exec, %4\n\tbuffer_load_dwordx4 %1, %2, %3 offen lds\n\ts_mov_b64 exec, -1"
                 ::"s"(lds), "v"(voff), "s"(srd), "s"(soff), "s"(mask) : "memory");
  else
    asm volatile("s_mov_b32 m0, %0\n\ts_mov_b64 exec, %4\n\tbuffer_load_dwordx4 %1, %2, %3 offen sc1 lds\n\ts_mov_b64 exec, -1"
                 ::"s"(lds), "v"(voff), "s"(srd), "s"(soff), "s"(mask) : "memory");
}
DEVI unsigned lds_addr(const char* p) { return (unsigned)(size_t)(const LAS char*)p; }
template <int N> DEVI void wait_bar() { asm volatile("s_waitcnt vmcnt(%0) lgkmcnt(0)\n\ts_barrier" ::"i"(N) : "memory"); }
DEVI void lds_bar() { asm volatile("s_waitcnt lgkmcnt(0)\n\ts_barrier" ::: "memory"); }
DEVI bf16x8 tr2(const char* a, const char* b) {
  s16x4 lo = __builtin_amdgcn_ds_read_tr16_b64_v4i16((LAS s16x4*)a);
  s16x4 hi = __builtin_amdgcn_ds_read_tr16_b64_v4i16((LAS s16x4*)b);
  return __builtin_bit_cast(bf16x8, __builtin_shufflevector(lo, hi, 0, 1, 2, 3, 4, 5, 6, 7));
}
DEVI bf16x8 lds_frag(const char* a) { return *(const LAS bf16x8*)a; }
DEVI f32x4 mfma(bf16x8 a, bf16x8 b, f32x4 c) { return __builtin_amdgcn_mfma_f32_16x16x32_bf16(a, b, c, 0, 0, 0); }
DEVI unsigned pk(float a, float b) { f32x2 v = {a, b}; return __builtin_bit_cast(unsigned, __builtin_convertvector(v, bf16x2)); }
DEVI bf16x8 mk_frag(unsigned a, unsigned b, unsigned c, unsigned d) { u32x4 v = {a, b, c, d}; return __builtin_bit_cast(bf16x8, v); }
DEVI float lo_f(unsigned u) { return __uint_as_float(u << 16); }
DEVI float hi_f(unsigned u) { return __uint_as_float(u & 0xffff0000u); }
DEVI float xor16(float v, int l) { auto r = __builtin_amdgcn_permlane16_swap(__float_as_uint(v), __float_as_uint(v), false, false); return __uint_as_float((l & 16) ? r[0] : r[1]); }
DEVI float red_max(float v) {
  auto r = __builtin_amdgcn_permlane16_swap(__float_as_uint(v), __float_as_uint(v), false, false);
  v = fmaxf(__uint_as_float(r[0]), __uint_as_float(r[1]));
  auto q = __builtin_amdgcn_permlane32_swap(__float_as_uint(v), __float_as_uint(v), false, false);
  return fmaxf(__uint_as_float(q[0]), __uint_as_float(q[1]));
}
DEVI float red_sum(float v) {
  auto r = __builtin_amdgcn_permlane16_swap(__float_as_uint(v), __float_as_uint(v), false, false);
  v = __uint_as_float(r[0]) + __uint_as_float(r[1]);
  auto q = __builtin_amdgcn_permlane32_swap(__float_as_uint(v), __float_as_uint(v), false, false);
  return __uint_as_float(q[0]) + __uint_as_float(q[1]);
}
DEVI u64 next_tag(u64 e) { return e * 6364136223846793005ull + 1442695040888963407ull; }
DEVI u64 ld_agent(const char* p) { return __hip_atomic_load((const GAS u64*)p, __ATOMIC_RELAXED, __HIP_MEMORY_SCOPE_AGENT); }
DEVI void st_agent(char* p, u64 v) { __hip_atomic_store((GAS u64*)p, v, __ATOMIC_RELAXED, __HIP_MEMORY_SCOPE_AGENT); }
DEVI void poll_flags(const char* flag, bool active, u64 tag) {
  for (unsigned spin = 0; spin < (1u << 20); ++spin) {
    bool ok = true;
    if (active) ok = ld_agent(flag) == tag;
    if (__all(ok)) break;
    __builtin_amdgcn_s_sleep(1);
  }
}

DEVI void dual_softmax(const f32x4 a, const float b, unsigned out[5]) {
  constexpr float L2E = 1.4426950408889634f;
  float mx = fmaxf(fmaxf(fmaxf(a[0], a[1]), fmaxf(a[2], a[3])), b);
  float mn = fminf(fminf(fminf(a[0], a[1]), fminf(a[2], a[3])), b);
  mx = red_max(mx);
  mn = -red_max(-mn);
  const float cp = -mx * L2E, cn = mn * L2E;
  float ep[5], en[5];
#pragma unroll
  for (int i = 0; i < 5; ++i) {
    const float v = i < 4 ? a[i] : b;
    ep[i] = __builtin_amdgcn_exp2f(fmaf(v, L2E, cp));
    en[i] = __builtin_amdgcn_exp2f(fmaf(v, -L2E, cn));
  }
  const float sp = red_sum((ep[0] + ep[1]) + (ep[2] + ep[3]) + ep[4]);
  const float sn = red_sum((en[0] + en[1]) + (en[2] + en[3]) + en[4]);
  const float rp = __builtin_amdgcn_rcpf(sp), rn = __builtin_amdgcn_rcpf(sn);
  out[0] = pk(ep[0] * rp, ep[1] * rp);
  out[1] = pk(ep[2] * rp, ep[3] * rp);
  out[2] = pk(ep[4] * rp, en[0] * rn);
  out[3] = pk(en[1] * rn, en[2] * rn);
  out[4] = pk(en[3] * rn, en[4] * rn);
}

constexpr int A_ST = 26624, A_R = 4;
constexpr int A_WQ = A_R * A_ST;
constexpr int A_PHI = A_WQ + 51264;
constexpr int A_CS = A_PHI + 1600;
constexpr int A_EP = A_CS + 512;
constexpr int A_LDS = A_EP + 16;
constexpr int A_TOK = 0;
constexpr int A_KVS = 40960;

template <int OAUX>
DEVI void phase_a(const Params& P, char* sm, const int p, const int j) {
  const int tid = threadIdx.x, l = tid & 63, w = __builtin_amdgcn_readfirstlane(tid >> 6);
  const int t = l & 15, g = l >> 4, q = (l & 15) >> 2, pp = l & 3;
  const u32x4 rx = mk_srd(P.x, S * D * 2), rw = mk_srd(P.wqkv, D * 3 * D * 2), rphi = mk_srd(P.wphi, 32 * 400 * 2);
  const rsrc_t rws = mk_rsrc(P.ws, WS_END);
  const unsigned sb = lds_addr(sm);

  if (w < 2) dma16m(rphi, sb + A_PHI + w * 1024, tid * 16, j * 1600, w == 0 ? ~0ull : (1ull << 36) - 1);
  if (w == 2) dma16m<16>(mk_srd(P.ws, WS_END), sb + A_EP, l * 16, WS_EPOCH + j * 256, 1ull);

  unsigned xoff[2], woff[2];
#pragma unroll
  for (int i = 0; i < 2; ++i) {
    xoff[i] = (p * 128 + 16 * w + 8 * i + (l >> 3)) * 1280 + (((l & 7) ^ ((4 * i + (l >> 4)) & 7)) * 16);
    const int slot = 8 * w + 4 * i + l / 10, ch = l % 10;
    const int r = (slot & ~12) | ((slot & 4) << 1) | ((slot & 8) >> 1);
    woff[i] = r * 3840 + (ch < 5 ? 1280 + 16 * ch : 2560 + 16 * (ch - 5)) + 80 * j;
  }
  auto issue = [&](int s) {
    const unsigned st = sb + (s % A_R) * A_ST;
    dma16<0>(rx, st + w * 2048, xoff[0], s * 128);
    dma16<0>(rx, st + w * 2048 + 1024, xoff[1], s * 128);
    dma16m(rw, st + 16384 + w * 1280, woff[0], s * 64 * 3840, (1ull << 40) - 1);
    dma16m(rw, st + 16384 + w * 1280 + 640, woff[1], s * 64 * 3840, (1ull << 40) - 1);
  };
  auto issue_q = [&](int i) {
    const int e = i * 64 + l;
    const int slot = 80 * w + e / 5, ch = e % 5;
    const int s16 = slot & 15;
    const int r = (slot & ~15) | ((s16 & 8)) | ((s16 & 1) << 2) | ((s16 >> 1) & 3);
    dma16m(rw, sb + A_WQ + w * 6400 + i * 1024, r * 3840 + 16 * ch + 80 * j, 0, i < 6 ? ~0ull : 0xffffull);
  };
  issue(0); issue(1); issue(2);

  const bf16x8 ones = mk_frag(0x3F803F80u, 0x3F803F80u, 0x3F803F80u, 0x3F803F80u);
  const int xrd = w * 2048 + t * 128;
  const int xsw = (t >> 1) & 7;
  const int wrd = 16384 + (16 * (g >> 1) + 4 * (g & 1) + q) * 160 + pp * 8;
  const int wcs = wrd + (w < 4 ? w : 4) * 32;
  bf16x8 xf[20];
  f32x4 acc[5], accS = {0, 0, 0, 0}, accG = {0, 0, 0, 0}, accC = {0, 0, 0, 0};
#pragma unroll
  for (int b = 0; b < 5; ++b) acc[b] = (f32x4){0, 0, 0, 0};
  bf16x8 phiA[2][2];

#pragma unroll
  for (int s = 0; s < 10; ++s) {
    if (s < 8) wait_bar<8>();
    else if (s == 8) wait_bar<4 + 3>();
    else wait_bar<3 + 2>();
    if (s + 3 < 10) issue(s + 3);
    else if (s == 7) { issue_q(0); issue_q(1); issue_q(2); }
    else if (s == 8) { issue_q(3); issue_q(4); }
    else { issue_q(5); issue_q(6); }
    const char* st = sm + (s % A_R) * A_ST;
#pragma unroll
    for (int k = 0; k < 2; ++k) {
      const bf16x8 xv = lds_frag(st + xrd + (((4 * k + g) ^ xsw) * 16));
      xf[2 * s + k] = xv;
#pragma unroll
      for (int b = 0; b < 5; ++b) acc[b] = mfma(tr2(st + wrd + k * 5120 + b * 32, st + wrd + k * 5120 + 1280 + b * 32), xv, acc[b]);
      accC = mfma(tr2(st + wcs + k * 5120, st + wcs + k * 5120 + 1280), ones, accC);
      accS = mfma(ones, xv, accS);
      accG = mfma(xv, xv, accG);
    }
    if (s == 0) {
#pragma unroll
      for (int hh = 0; hh < 2; ++hh)
#pragma unroll
        for (int ob = 0; ob < 2; ++ob) {
          unsigned v[4];
#pragma unroll
          for (int jj = 0; jj < 8; ++jj) {
            const int col = 16 * (hh + (jj >> 2)) + 4 * g + (jj & 3);
            const int d = col - 20 * hh;
            const int f = ob == 0 ? t : 16 + (t >> 2);
            const bool ok = d >= 0 && d < 20 && (ob == 0 || (t & 3) == 0);
            const unsigned e = *(const LAS u16*)(sm + A_PHI + hh * 800 + (ok ? d * 40 + f * 2 : 0));
            const unsigned m = ok ? e : 0u;
            if (jj & 1) v[jj >> 1] |= m << 16; else v[jj >> 1] = m;
          }
          phiA[hh][ob] = mk_frag(v[0], v[1], v[2], v[3]);
        }
    }
  }

  if (t == 0 && w < 5) *(LAS f32x4*)(sm + A_CS + w * 64 + g * 16) = accC;
  const float mean = accS[0] * (1.f / D);
  float dg = (t & 2) ? ((t & 1) ? accG[3] : accG[2]) : ((t & 1) ? accG[1] : accG[0]);
  dg = __shfl(dg, (t >> 2) * 16 + t);
  const float rstd = __builtin_amdgcn_rsqf(fmaxf(dg * (1.f / D) - mean * mean, 0.f) + 1e-5f);
  lds_bar();

  {
    unsigned pkd[5][2];
#pragma unroll
    for (int b = 0; b < 5; ++b) {
      const f32x4 cs = *(const LAS f32x4*)(sm + A_CS + b * 64 + g * 16);
      float v[4];
#pragma unroll
      for (int r = 0; r < 4; ++r) v[r] = (acc[b][r] - mean * cs[r]) * rstd;
      pkd[b][0] = pk(v[0], v[1]); pkd[b][1] = pk(v[2], v[3]);
    }
    char* row = sm + A_TOK + (16 * w + t) * 288;
#pragma unroll
    for (int hh = 0; hh < 2; ++hh) {
      const bf16x8 kb = mk_frag(pkd[hh][0], pkd[hh][1], pkd[hh + 1][0], pkd[hh + 1][1]);
      const f32x4 z = {0, 0, 0, 0};
      const f32x4 p0 = mfma(phiA[hh][0], kb, z), p1 = mfma(phiA[hh][1], kb, z);
      unsigned o[5];
      dual_softmax(p0, p1[0], o);
      *(LAS u32x2*)(row + hh * 96 + g * 8) = (u32x2){o[0], o[1]};
      *(LAS u32x2*)(row + hh * 96 + 32 + g * 8) = (u32x2){o[2], o[3]};
      *(LAS u32x2*)(row + hh * 96 + 64 + g * 8) = (u32x2){o[4], 0u};
    }
    if (g >= 2) *(LAS u32x2*)(row + 192 + (g - 2) * 8) = (u32x2){pkd[2][0], pkd[2][1]};
    else *(LAS u32x2*)(row + 272 + g * 8) = (u32x2){0x3F80u, 0u};
    *(LAS u32x2*)(row + 192 + (2 + g) * 8) = (u32x2){pkd[3][0], pkd[3][1]};
    *(LAS u32x2*)(row + 192 + (6 + g) * 8) = (u32x2){pkd[4][0], pkd[4][1]};
  }
  lds_bar();

  if (w < 4) {
    const int hh = w >> 1, db = w & 1;
    const char* base = sm + A_TOK + (4 * g + q) * 288;
    const char* pa = base + hh * 96 + pp * 8;
    const int vg = db == 0 ? 5 * hh + pp : (pp == 0 ? 5 * hh + 4 : 10);
    const char* pb = base + 192 + vg * 8;
    f32x4 kv[3] = {{0, 0, 0, 0}, {0, 0, 0, 0}, {0, 0, 0, 0}};
#pragma unroll
    for (int k = 0; k < 4; ++k) {
      const bf16x8 vb = tr2(pb + k * 32 * 288, pb + (k * 32 + 16) * 288);
#pragma unroll
      for (int b = 0; b < 3; ++b) kv[b] = mfma(tr2(pa + k * 32 * 288 + b * 32, pa + (k * 32 + 16) * 288 + b * 32), vb, kv[b]);
    }
    const unsigned dst = WS_KVP + (j * 16 + p) * KVP_SZ;
    const int item = hh * 84 + (db == 0 ? l : 64 + g * 5 + t);
    if (db == 0 || t < 5) {
      const u32x4 m = {pk(kv[0][0], kv[0][1]), pk(kv[0][2], kv[0][3]), pk(kv[1][0], kv[1][1]), pk(kv[1][2], kv[1][3])};
      __builtin_amdgcn_raw_buffer_store_b128(m, rws, item * 16, dst, 16);
      __builtin_amdgcn_raw_buffer_store_b32(pk(kv[2][0], kv[2][1]), rws, 2688 + item * 4, dst, 16);
    }
  }
  const u64 tag = next_tag(*(const LAS u64*)(sm + A_EP));
  wait_bar<0>();
  if (tid == 0) st_agent(P.ws + WS_FKV + (j * 16 + p) * 128, tag);

  f32x4 aq[3] = {{0, 0, 0, 0}, {0, 0, 0, 0}, {0, 0, 0, 0}};
  {
    const char* qrd = sm + A_WQ + (16 * (g >> 1) + 2 * (4 * (g & 1) + q)) * 80 + pp * 8;
    f32x4 cq = {0, 0, 0, 0};
#pragma unroll
    for (int k = 0; k < 20; ++k) {
#pragma unroll
      for (int b = 0; b < 3; ++b) aq[b] = mfma(tr2(qrd + k * 2560 + b * 32, qrd + k * 2560 + 80 + b * 32), xf[k], aq[b]);
    }
    if (w < 3) {
#pragma unroll
      for (int k = 0; k < 20; ++k) cq = mfma(tr2(qrd + k * 2560 + w * 32, qrd + k * 2560 + 80 + w * 32), ones, cq);
      if (t == 0) *(LAS f32x4*)(sm + A_CS + 320 + w * 64 + g * 16) = cq;
    }
  }
  if (w == 7) {
    poll_flags(P.ws + WS_FKV + (j * 16 + (l & 15)) * 128, l < 16, tag);
    if (l == 0) st_agent(P.ws + WS_EPOCH + j * 256, tag);
  }
  lds_bar();

  u32x4 part[8];
  const int item = tid & 255, half = tid >> 8;
  if (item < 210) {
#pragma unroll
    for (int i = 0; i < 8; ++i) part[i] = __builtin_amdgcn_raw_buffer_load_b128(rws, item * 16, WS_KVP + (j * 16 + (w >> 2) * 8 + i) * KVP_SZ, 16);
  }
  unsigned fq[2][5];
  {
    unsigned pkd[3][2];
#pragma unroll
    for (int b = 0; b < 3; ++b) {
      const f32x4 cs = *(const LAS f32x4*)(sm + A_CS + 320 + b * 64 + g * 16);
      float v[4];
#pragma unroll
      for (int r = 0; r < 4; ++r) v[r] = (aq[b][r] - mean * cs[r]) * rstd;
      pkd[b][0] = pk(v[0], v[1]); pkd[b][1] = pk(v[2], v[3]);
    }
    if (g >= 2) { pkd[2][0] = 0u; pkd[2][1] = 0u; }
#pragma unroll
    for (int hh = 0; hh < 2; ++hh) {
      const bf16x8 qb = mk_frag(pkd[hh][0], pkd[hh][1], pkd[hh + 1][0], pkd[hh + 1][1]);
      const f32x4 z = {0, 0, 0, 0};
      const f32x4 p0 = mfma(phiA[hh][0], qb, z), p1 = mfma(phiA[hh][1], qb, z);
      dual_softmax(p0, p1[0], fq[hh]);
    }
  }
  if (item < 210) {
    float sum[8];
#pragma unroll
    for (int c = 0; c < 4; ++c) { sum[2 * c] = lo_f(part[0][c]); sum[2 * c + 1] = hi_f(part[0][c]); }
#pragma unroll
    for (int i = 1; i < 8; ++i)
#pragma unroll
      for (int c = 0; c < 4; ++c) { sum[2 * c] += lo_f(part[i][c]); sum[2 * c + 1] += hi_f(part[i][c]); }
    *(LAS u32x4*)(sm + A_KVS + half * KVP_SZ + item * 16) = (u32x4){pk(sum[0], sum[1]), pk(sum[2], sum[3]), pk(sum[4], sum[5]), pk(sum[6], sum[7])};
  }
  lds_bar();

  {
    unsigned om[4], ol[4];
#pragma unroll
    for (int hh = 0; hh < 2; ++hh) {
      const bf16x8 b0 = mk_frag(fq[hh][0], fq[hh][1], fq[hh][2], fq[hh][3]);
      const bf16x8 b1 = mk_frag(fq[hh][4], fq[hh][4], 0u, 0u);
      f32x4 n[2];
#pragma unroll
      for (int db = 0; db < 2; ++db) {
        const int it = hh * 84 + (db == 0 ? l : 64 + g * 5 + (t < 5 ? t : 4));
        const char* s0 = sm + A_KVS + it * 16;
        const char* s1 = sm + A_KVS + 2688 + it * 4;
        f32x4 c = {0, 0, 0, 0};
        c = mfma(lds_frag(s0), b0, c);
        c = mfma(lds_frag(s0 + KVP_SZ), b0, c);
        c = mfma(mk_frag(*(const LAS unsigned*)s1, *(const LAS unsigned*)(s1 + KVP_SZ), 0u, 0u), b1, c);
        n[db] = c;
      }
      const float den = __shfl(n[1][0], 16 + t) + 1e-6f;
      const float rd = __builtin_amdgcn_rcpf(den);
      om[2 * hh] = pk(n[0][0] * rd, n[0][1] * rd); om[2 * hh + 1] = pk(n[0][2] * rd, n[0][3] * rd);
      ol[2 * hh] = pk(n[1][0] * rd, n[1][1] * rd); ol[2 * hh + 1] = pk(n[1][2] * rd, n[1][3] * rd);
    }
    const unsigned tb = WS_O + (p * 8 + w) * 20 * 1024;
    __builtin_amdgcn_raw_buffer_store_b128((u32x4){om[0], om[1], om[2], om[3]}, rws, l * 16, tb + j * 1024, OAUX);
    if (g == 0) __builtin_amdgcn_raw_buffer_store_b128((u32x4){ol[0], ol[1], ol[2], ol[3]}, rws, t * 16, tb + (16 + (j >> 2)) * 1024 + (j & 3) * 256, OAUX);
  }
}

constexpr int G_ST = 20480, G_R = 6;
constexpr int G_LDS = G_R * G_ST;

template <int NKC, bool IS_B, int AAUX>
DEVI void gemm64(char* sm, u32x4 ra, unsigned abase, u32x4 rw, int ct, int nb, f32x4 (&acc)[3]) {
  const int tid = threadIdx.x, l = tid & 63, w = __builtin_amdgcn_readfirstlane(tid >> 6);
  const int g = l >> 4, q = (l & 15) >> 2, pp = l & 3;
  constexpr int NS = NKC / 2;
  const bool hasA = w < nb;
  const unsigned aoff = abase + w * NKC * 1024;
  const unsigned sb = lds_addr(sm);
  const int i = 8 * (w & 3) + (l >> 3), hj = i >> 4, gg = (i >> 2) & 3, e = i & 3, kcl = w >> 2;
  const unsigned wcol = ct * 128 + (((l & 7) ^ (((i >> 1) & 3) << 1)) * 16);
  unsigned woff, woff2 = 0;
  if (IS_B) {
    woff = (40 * kcl + 20 * hj + 4 * gg + e) * 1280 + wcol;
    woff2 = (40 * (4 * kcl + gg) + 20 * hj + 16 + e) * 1280 + wcol;
  } else {
    woff = (32 * kcl + 16 * (gg >> 1) + 4 * (gg & 1) + 8 * hj + e) * 1280 + wcol;
  }
  auto issue = [&](int s, int slot) {
    const unsigned st = sb + slot * G_ST;
    if (IS_B) dma16<0>(rw, st + 12288 + w * 1024, s < 8 ? woff : woff2, s < 8 ? s * 80 * 1280 : (s - 8) * 320 * 1280);
    else dma16<0>(rw, st + 12288 + w * 1024, woff, s * 64 * 1280);
    if (hasA) {
      dma16<AAUX>(ra, st + w * 2048, l * 16, aoff + s * 2048);
      dma16<AAUX>(ra, st + w * 2048 + 1024, l * 16, aoff + s * 2048 + 1024);
    }
  };
#pragma unroll
  for (int s = 0; s < G_R - 1; ++s) issue(s, s);
  const int mh = w >> 2, cb = w & 3;
  const int ard = mh * 6144 + l * 16;
  const int wrd = 12288 + (4 * g + q) * 128 + ((cb ^ ((2 * g + (q >> 1)) & 3)) * 32) + pp * 8;
  struct Frags { bf16x8 w[2], a[2][3]; };
  auto rd = [&](Frags& f, int slot) {
    const char* st = sm + slot * G_ST;
#pragma unroll
    for (int k = 0; k < 2; ++k) {
      f.w[k] = tr2(st + wrd + k * 4096, st + wrd + k * 4096 + 2048);
#pragma unroll
      for (int m = 0; m < 3; ++m) f.a[k][m] = lds_frag(st + ard + m * 2048 + k * 1024);
    }
  };
  auto mm = [&](const Frags& f) {
#pragma unroll
    for (int k = 0; k < 2; ++k)
#pragma unroll
      for (int m = 0; m < 3; ++m) acc[m] = mfma(f.w[k], f.a[k][m], acc[m]);
  };
  int rslot = 1, islot = G_R - 1;
  auto step = [&](int s, const Frags& cur, Frags& nxt) {
    if (s + G_R - 2 < NS) { if (hasA) wait_bar<3 * (G_R - 3)>(); else wait_bar<(G_R - 3)>(); }
    else wait_bar<0>();
    if (s + G_R - 1 < NS) issue(s + G_R - 1, islot);
    rd(nxt, rslot);
    mm(cur);
    rslot = rslot + 1 == G_R ? 0 : rslot + 1;
    islot = islot + 1 == G_R ? 0 : islot + 1;
  };
  Frags f0, f1;
  if (hasA) wait_bar<3 * (G_R - 2)>(); else wait_bar<(G_R - 2)>();
  rd(f0, 0);
#pragma unroll 1
  for (int s = 0; s + 2 < NS; s += 2) { step(s, f0, f1); step(s + 1, f1, f0); }
  if (NS % 2 == 0) { step(NS - 2, f0, f1); mm(f1); }
  else mm(f0);
}

DEVI bool tile_bd(int& rt, int& ct, int& tb0, int& nb) {
  const int b = blockIdx.x, xcd = b & 7, qn = (xcd >> 1) * 32 + (b >> 3);
  if (qn >= 125) return false;
  rt = qn / 5; ct = 5 * (xcd & 1) + qn % 5;
  tb0 = rt * 128 / 25; nb = (rt + 1) * 128 / 25 - tb0;
  return true;
}

template <int AUX>
DEVI void phase_b(const Params& P, char* sm, int ct, int tb0, int nb) {
  const int tid = threadIdx.x, l = tid & 63, w = __builtin_amdgcn_readfirstlane(tid >> 6);
  const int t = l & 15, g = l >> 4, mh = w >> 2, cb = w & 3;
  const rsrc_t rws = mk_rsrc(P.ws, WS_END), rx = mk_rsrc(P.x, S * D * 2);
  u32x2 xr[3];
#pragma unroll
  for (int m = 0; m < 3; ++m)
    xr[m] = __builtin_amdgcn_raw_buffer_load_b64(rx, ((tb0 + 3 * mh + m) * 16 + t) * 1280 + ct * 128 + cb * 32 + g * 8, 0, 0);
  f32x4 acc[3] = {{0, 0, 0, 0}, {0, 0, 0, 0}, {0, 0, 0, 0}};
  gemm64<20, true, AUX>(sm, mk_srd(P.ws, WS_END), WS_O + tb0 * 20 * 1024, mk_srd(P.wo, D * D * 2), ct, nb, acc);
  lds_bar();
  unsigned hp[3][2];
#pragma unroll
  for (int m = 0; m < 3; ++m) {
    hp[m][0] = pk(acc[m][0] + lo_f(xr[m][0]), acc[m][1] + hi_f(xr[m][0]));
    hp[m][1] = pk(acc[m][2] + lo_f(xr[m][1]), acc[m][3] + hi_f(xr[m][1]));
    const float a = lo_f(hp[m][0]), b = hi_f(hp[m][0]), c = lo_f(hp[m][1]), d = hi_f(hp[m][1]);
    const float s1 = red_sum((a + b) + (c + d));
    const float s2 = red_sum(fmaf(a, a, b * b) + fmaf(c, c, d * d));
    if (g == 0) *(LAS f32x2*)(sm + ((3 * mh + m) * 16 + t) * 32 + cb * 8) = (f32x2){s1, s2};
  }
  const unsigned hbase = WS_H + ((tb0 + 3 * mh) * 20 + 2 * ct + (cb >> 1)) * 1024 + (cb & 1) * 512;
  {
    auto r0 = __builtin_amdgcn_permlane32_swap(hp[0][0], hp[1][0], false, false);
    auto r1 = __builtin_amdgcn_permlane32_swap(hp[0][1], hp[1][1], false, false);
    __builtin_amdgcn_raw_buffer_store_b128((u32x4){r0[0], r1[0], r0[1], r1[1]}, rws, (l & 31) * 16 + (l >> 5) * 20 * 1024, hbase, AUX);
    auto r2 = __builtin_amdgcn_permlane32_swap(hp[2][0], hp[2][0], false, false);
    auto r3 = __builtin_amdgcn_permlane32_swap(hp[2][1], hp[2][1], false, false);
    if (l < 32 && 3 * mh + 2 < nb) __builtin_amdgcn_raw_buffer_store_b128((u32x4){r2[0], r3[0], r2[1], r3[1]}, rws, l * 16, hbase + 2 * 20 * 1024, AUX);
  }
  lds_bar();
  if (tid < nb * 16) {
    const f32x4 a = *(const LAS f32x4*)(sm + tid * 32), b = *(const LAS f32x4*)(sm + tid * 32 + 16);
    const f32x2 r = {(a[0] + a[2]) + (b[0] + b[2]), (a[1] + a[3]) + (b[1] + b[3])};
    __builtin_amdgcn_raw_buffer_store_b64(__builtin_bit_cast(u32x2, r), rws, tid * 8, WS_ST + (ct * 2048 + tb0 * 16) * 8, AUX);
  }
}

template <int AUX>
DEVI void phase_d(const Params& P, char* sm, int ct, int tb0, int nb) {
  const int tid = threadIdx.x, l = tid & 63, w = __builtin_amdgcn_readfirstlane(tid >> 6);
  const int t = l & 15, g = l >> 4, mh = w >> 2, cb = w & 3;
  const rsrc_t rws = mk_rsrc(P.ws, WS_END), ro = mk_rsrc(P.out, S * D * 2);
  u32x2 hr[3];
#pragma unroll
  for (int m = 0; m < 3; ++m)
    hr[m] = __builtin_amdgcn_raw_buffer_load_b64(rws, ((g & 1) * 16 + t) * 16 + (g >> 1) * 8,
                                                 WS_H + ((tb0 + 3 * mh + m) * 20 + 2 * ct + (cb >> 1)) * 1024 + (cb & 1) * 512, AUX);
  f32x4 acc[3] = {{0, 0, 0, 0}, {0, 0, 0, 0}, {0, 0, 0, 0}};
  gemm64<54, false, AUX>(sm, mk_srd(P.ws, WS_END), WS_ACT + tb0 * 54 * 1024, mk_srd(P.wd, DF * D * 2), ct, nb, acc);
  lds_bar();
#pragma unroll
  for (int m = 0; m < 3; ++m) {
    const u32x2 v = {pk(acc[m][0] + lo_f(hr[m][0]), acc[m][1] + hi_f(hr[m][0])), pk(acc[m][2] + lo_f(hr[m][1]), acc[m][3] + hi_f(hr[m][1]))};
    *(LAS u32x2*)(sm + ((3 * mh + m) * 16 + t) * 144 + cb * 32 + g * 8) = v;
  }
  lds_bar();
#pragma unroll
  for (int i = 0; i < 2; ++i) {
    const int c = tid + i * NT, row = c >> 3;
    if (row < nb * 16) {
      const u32x4 v = *(const LAS u32x4*)(sm + row * 144 + (c & 7) * 16);
      __builtin_amdgcn_raw_buffer_store_b128(v, ro, (tb0 * 16 + row) * 1280 + ct * 128 + (c & 7) * 16, 0, 0);
    }
  }
}

constexpr int C_ST = 23552, C_R = 6;
constexpr int C_STAT = C_R * C_ST;
constexpr int C_CS = C_STAT + 2048;
constexpr int C_ONE = C_CS + 512;
constexpr int C_LDS = C_ONE + 1024;

DEVI bool tile_c(int& ct, int& tb0, int& nb) {
  const int b = blockIdx.x, xcd = b & 7, idx = b >> 3;
  const int lo = xcd * 243 / 8, hi = (xcd + 1) * 243 / 8, qn = lo + idx;
  if (qn >= hi) return false;
  const int band = qn / 81, rem = qn % 81, rt = band * 3 + rem % 3;
  ct = rem / 3;
  tb0 = rt * 128 / 9; nb = (rt + 1) * 128 / 9 - tb0;
  return true;
}

template <int AUX>
DEVI void phase_c(const Params& P, char* sm, int ct, int tb0, int nb) {
  const int tid = threadIdx.x, l = tid & 63, w = __builtin_amdgcn_readfirstlane(tid >> 6);
  const int t = l & 15, g = l >> 4, q = (l & 15) >> 2, pp = l & 3, mh = w >> 2, nq = w & 3;
  const rsrc_t rws = mk_rsrc(P.ws, WS_END);
  const u32x4 rw = mk_srd(mh ? P.wu : P.wg, D * DF * 2), ra = mk_srd(P.ws, WS_END);
  const unsigned sb = lds_addr(sm);
  const bool two = w + 8 < nb;
  const unsigned aoff = WS_H + (tb0 + w) * 20 * 1024;
  const int i = 8 * (w & 3) + (l >> 3), hj = i >> 4, gg = (i >> 2) & 3, e = i & 3;
  const unsigned woff = (16 * (gg >> 1) + 4 * (gg & 1) + 8 * hj + e) * 3456 + ct * 128 + (((l & 7) ^ (((i >> 1) & 3) << 1)) * 16);
  auto issue = [&](int s, int slot) {
    const unsigned st = sb + slot * C_ST;
    dma16<0>(rw, st + 15360 + w * 1024, woff, s * 32 * 3456);
    dma16<AUX>(ra, st + w * 1024, l * 16, aoff + s * 1024);
    if (two) dma16<AUX>(ra, st + (w + 8) * 1024, l * 16, aoff + 8 * 20 * 1024 + s * 1024);
  };
  f32x2 sp[10];
  if (tid < nb * 16) {
#pragma unroll
    for (int c = 0; c < 10; ++c) sp[c] = __builtin_bit_cast(f32x2, __builtin_amdgcn_raw_buffer_load_b64(rws, tid * 8, WS_ST + (c * 2048 + tb0 * 16) * 8, AUX));
  }
#pragma unroll
  for (int s = 0; s < C_R - 1; ++s) issue(s, s);
  if (w == 7) *(LAS u32x4*)(sm + C_ONE + l * 16) = (u32x4){0x3F803F80u, 0x3F803F80u, 0x3F803F80u, 0x3F803F80u};
  const int b0 = mh ? nb - 7 : 0;
  const bool eight = mh == 0 && nb == 15;
  const int ard = b0 * 1024 + l * 16;
  const int wrd = 15360 + (4 * g + q) * 128 + ((nq ^ ((2 * g + (q >> 1)) & 3)) * 32) + pp * 8;
  f32x4 ag[8], au[8];
#pragma unroll
  for (int m = 0; m < 8; ++m) { ag[m] = (f32x4){0, 0, 0, 0}; au[m] = (f32x4){0, 0, 0, 0}; }
  struct Frags { bf16x8 g, u, a[8]; };
  auto rd = [&](Frags& f, int slot) {
    const char* st = sm + slot * C_ST;
    f.g = tr2(st + wrd, st + wrd + 2048);
    f.u = tr2(st + wrd + 4096, st + wrd + 6144);
#pragma unroll
    for (int m = 0; m < 7; ++m) f.a[m] = lds_frag(st + ard + m * 1024);
    f.a[7] = lds_frag(sm + (mh ? C_ONE : slot * C_ST + 7 * 1024) + l * 16);
  };
  auto mm = [&](const Frags& f) {
#pragma unroll
    for (int m = 0; m < 8; ++m) { ag[m] = mfma(f.g, f.a[m], ag[m]); au[m] = mfma(f.u, f.a[m], au[m]); }
  };
  int rslot = 1, islot = C_R - 1;
  auto step = [&](int s, const Frags& cur, Frags& nxt) {
    if (s + C_R - 2 < 20) { if (two) wait_bar<3 * (C_R - 3)>(); else wait_bar<2 * (C_R - 3)>(); }
    else wait_bar<0>();
    if (s + C_R - 1 < 20) issue(s + C_R - 1, islot);
    rd(nxt, rslot);
    mm(cur);
    rslot = rslot + 1 == C_R ? 0 : rslot + 1;
    islot = islot + 1 == C_R ? 0 : islot + 1;
  };
  Frags f0, f1;
  if (two) wait_bar<3 * (C_R - 2)>(); else wait_bar<2 * (C_R - 2)>();
  rd(f0, 0);
#pragma unroll 1
  for (int s = 0; s + 2 < 20; s += 2) { step(s, f0, f1); step(s + 1, f1, f0); }
  step(18, f0, f1);
  mm(f1);
  if (tid < nb * 16) {
    float s1 = 0.f, s2 = 0.f;
#pragma unroll
    for (int c = 0; c < 10; ++c) { s1 += sp[c][0]; s2 += sp[c][1]; }
    const float mean = s1 * (1.f / D);
    const float rstd = __builtin_amdgcn_rsqf(fmaxf(s2 * (1.f / D) - mean * mean, 0.f) + 1e-5f);
    *(LAS f32x2*)(sm + C_STAT + tid * 8) = (f32x2){mean, rstd};
  }
  if (mh && t == 0) {
    *(LAS f32x4*)(sm + C_CS + nq * 128 + g * 16) = ag[7];
    *(LAS f32x4*)(sm + C_CS + nq * 128 + 64 + g * 16) = au[7];
  }
  lds_bar();
  const f32x4 csg = *(const LAS f32x4*)(sm + C_CS + nq * 128 + g * 16), csu = *(const LAS f32x4*)(sm + C_CS + nq * 128 + 64 + g * 16);
  constexpr float C0 = -2.f * 0.7978845608028654f * 1.4426950408889634f, C1 = C0 * 0.044715f;
  unsigned ap[8][2];
#pragma unroll
  for (int m = 0; m < 8; ++m) {
    if (m < 7 || eight) {
      const f32x2 st = *(const LAS f32x2*)(sm + C_STAT + ((b0 + m) * 16 + t) * 8);
      float o[4];
#pragma unroll
      for (int r = 0; r < 4; ++r) {
        const float gv = (ag[m][r] - st[0] * csg[r]) * st[1];
        const float uv = (au[m][r] - st[0] * csu[r]) * st[1];
        const float ex = __builtin_amdgcn_exp2f(gv * fmaf(gv * gv, C1, C0));
        o[r] = gv * uv * __builtin_amdgcn_rcpf(1.f + ex);
      }
      ap[m][0] = pk(o[0], o[1]); ap[m][1] = pk(o[2], o[3]);
    } else { ap[m][0] = 0u; ap[m][1] = 0u; }
  }
  const unsigned base = WS_ACT + ((tb0 + b0) * 54 + 2 * ct + (nq >> 1)) * 1024 + (nq & 1) * 512;
#pragma unroll
  for (int m = 0; m < 8; m += 2) {
    auto r0 = __builtin_amdgcn_permlane32_swap(ap[m][0], ap[m + 1][0], false, false);
    auto r1 = __builtin_amdgcn_permlane32_swap(ap[m][1], ap[m + 1][1], false, false);
    if (m < 6 || eight || l < 32)
      __builtin_amdgcn_raw_buffer_store_b128((u32x4){r0[0], r1[0], r0[1], r1[1]}, rws, (l & 31) * 16 + (l >> 5) * 54 * 1024, base + m * 54 * 1024, AUX);
  }
}

extern __shared__ __attribute__((aligned(16))) char smem[];

__global__ __launch_bounds__(NT, 2) void k_a(Params P) {
  const int b = blockIdx.x, xcd = b & 7, idx = b >> 3;
  phase_a<0>(P, smem, 4 * (xcd >> 1) + (idx >> 3), 8 * (xcd & 1) + (idx & 7));
}
__global__ __launch_bounds__(NT, 2) void k_b(Params P) {
  int rt, ct, tb0, nb;
  if (!tile_bd(rt, ct, tb0, nb)) return;
  phase_b<0>(P, smem, ct, tb0, nb);
}
__global__ __launch_bounds__(NT, 2) void k_c(Params P) {
  int ct, tb0, nb;
  if (!tile_c(ct, tb0, nb)) return;
  phase_c<0>(P, smem, ct, tb0, nb);
}
__global__ __launch_bounds__(NT, 2) void k_d(Params P) {
  int rt, ct, tb0, nb;
  if (!tile_bd(rt, ct, tb0, nb)) return;
  phase_d<0>(P, smem, ct, tb0, nb);
}

extern "C" void kernel_launch(void* const* d_in, const int* in_sizes, int n_in, void* d_out, int out_size, void* d_ws, size_t ws_size,
                              hipStream_t stream) {
  Params P;
  P.x = (const u16*)d_in[0]; P.wqkv = (const u16*)d_in[1]; P.wphi = (const u16*)d_in[2]; P.wo = (const u16*)d_in[3];
  P.wg = (const u16*)d_in[5]; P.wu = (const u16*)d_in[6]; P.wd = (const u16*)d_in[7];
  P.out = (u16*)d_out; P.ws = (char*)d_ws;
  (void)hipFuncSetAttribute((const void*)k_a, hipFuncAttributeMaxDynamicSharedMemorySize, A_LDS);
  (void)hipFuncSetAttribute((const void*)k_b, hipFuncAttributeMaxDynamicSharedMemorySize, G_LDS);
  (void)hipFuncSetAttribute((const void*)k_c, hipFuncAttributeMaxDynamicSharedMemorySize, C_LDS);
  (void)hipFuncSetAttribute((const void*)k_d, hipFuncAttributeMaxDynamicSharedMemorySize, G_LDS);
  k_a<<<256, NT, A_LDS, stream>>>(P);
  k_b<<<256, NT, G_LDS, stream>>>(P);
  k_c<<<256, NT, C_LDS, stream>>>(P);
  k_d<<<256, NT, G_LDS, stream>>>(P);
}
```

```cpp
#include <hip/hip_runtime.h>

typedef unsigned short u16;
typedef unsigned long long u64;
typedef __bf16 bf16x8 __attribute__((ext_vector_type(8)));
typedef __bf16 bf16x2 __attribute__((ext_vector_type(2)));
typedef short s16x4 __attribute__((ext_vector_type(4)));
typedef float f32x4 __attribute__((ext_vector_type(4)));
typedef float f32x2 __attribute__((ext_vector_type(2)));
typedef unsigned u32x4 __attribute__((ext_vector_type(4)));
typedef unsigned u32x2 __attribute__((ext_vector_type(2)));
typedef __amdgpu_buffer_rsrc_t rsrc_t;
#define LAS __attribute__((address_space(3)))
#define GAS __attribute__((address_space(1)))
#define DEVI __device__ __forceinline__

constexpr int S = 2048, D = 640, DF = 1728;
constexpr int NT = 768;
constexpr int SC1 = 16;
#define ROTA 1

constexpr unsigned WS_EPOCH = 0;
constexpr unsigned WS_FKV = 4096;
constexpr unsigned WS_FO = WS_FKV + 32768;
constexpr unsigned WS_FH = WS_FO + 32768;
constexpr unsigned WS_FA = WS_FH + 32768;
constexpr unsigned WS_KVP = 139264;
constexpr unsigned KVP_SZ = 3456;
constexpr unsigned WS_O = 1u << 20;
constexpr unsigned WS_H = WS_O + 128 * 20 * 1024;
constexpr unsigned WS_ST = WS_H + 128 * 20 * 1024;
constexpr unsigned WS_ACT = WS_ST + 10 * 2048 * 8;
constexpr unsigned WS_END = WS_ACT + 128 * 54 * 1024;

constexpr int AR = 0;
constexpr int WR = 61440, WR_N = 11;
constexpr int MISC = WR + WR_N * 8192;
constexpr int L_BAR = MISC + 48;
constexpr int L_SIG = MISC + 80;
constexpr int L_GO = MISC + 96;
constexpr int L_PUB = MISC + 112;
constexpr int L_EP = MISC + 128;
constexpr int L_CS = MISC + 256;
constexpr int L_PHI = L_CS + 512;
constexpr int L_STAT = L_PHI;
constexpr int L_PHF = L_PHI + 2048;
constexpr int L_KVS = L_PHF + 4096;
constexpr int L_ONE = L_KVS + KVP_SZ;
constexpr int LDS_BYTES = L_ONE + 1024;
static_assert(LDS_BYTES <= 163840, "LDS");
constexpr int A_ST = 26624;
constexpr int A_WQ = 0;
constexpr int A_TOK = 2 * A_ST;

#define STAMPT(T, ph, i)
#define STAMP(ph, i) STAMPT(0, ph, i)

struct Params {
  const u16* x; const u16* wqkv; const u16* wphi; const u16* wo;
  const u16* wg; const u16* wu; const u16* wd; u16* out; char* ws;
};
struct Tiles { int p, j; int hasB, ctB, tbB, nbB, rtB; int hasC, ctC, tbC, nbC, rtC; };

DEVI rsrc_t mk_rsrc(const void* p, unsigned bytes) { return __builtin_amdgcn_make_buffer_rsrc((void*)p, 0, bytes, 0x00020000); }
DEVI u32x4 mk_srd(const void* p, unsigned bytes) {
  const u64 a = (u64)p;
  return (u32x4){(unsigned)a, (unsigned)(a >> 32) & 0xffffu, bytes, 0x00020000u};
}
template <int AUX = 0> DEVI void dma16(u32x4 srd, unsigned lds, unsigned voff, unsigned soff) {
  if constexpr (AUX == 0)
    asm volatile("s_mov_b32 m0, %0\n\ts_nop 0\n\tbuffer_load_dwordx4 %1, %2, %3 offen lds" ::"s"(lds), "v"(voff), "s"(srd), "s"(soff) : "memory");
  else
    asm volatile("s_mov_b32 m0, %0\n\ts_nop 0\n\tbuffer_load_dwordx4 %1, %2, %3 offen sc1 lds" ::"s"(lds), "v"(voff), "s"(srd), "s"(soff) : "memory");
}
template <int AUX = 0> DEVI void dma16m(u32x4 srd, unsigned lds, unsigned voff, unsigned soff, u64 mask) {
  if constexpr (AUX == 0)
    asm volatile("s_mov_b32 m0, %0\n\ts_mov_b64 exec, %4\n\tbuffer_load_dwordx4 %1, %2, %3 offen lds\n\ts_mov_b64 exec, -1"
                 ::"s"(lds), "v"(voff), "s"(srd), "s"(soff), "s"(mask) : "memory");
  else
    asm volatile("s_mov_b32 m0, %0\n\ts_mov_b64 exec, %4\n\tbuffer_load_dwordx4 %1, %2, %3 offen sc1 lds\n\ts_mov_b64 exec, -1"
                 ::"s"(lds), "v"(voff), "s"(srd), "s"(soff), "s"(mask) : "memory");
}
DEVI unsigned lds_addr(const char* p) { return (unsigned)(size_t)(const LAS char*)p; }
template <int N> DEVI void wait_vm() { asm volatile("s_waitcnt vmcnt(%0)" ::"i"(N) : "memory"); }
template <int N> DEVI void wait_bar() { asm volatile("s_waitcnt vmcnt(%0)\n\ts_barrier" ::"i"(N) : "memory"); }
DEVI void lds_bar() { asm volatile("s_waitcnt lgkmcnt(0)\n\ts_barrier" ::: "memory"); }
DEVI void wait_lds() { asm volatile("s_waitcnt lgkmcnt(0)" ::: "memory"); }
DEVI bf16x8 tr2(const char* a, const char* b) {
  s16x4 lo = __builtin_amdgcn_ds_read_tr16_b64_v4i16((LAS s16x4*)a);
  s16x4 hi = __builtin_amdgcn_ds_read_tr16_b64_v4i16((LAS s16x4*)b);
  return __builtin_bit_cast(bf16x8, __builtin_shufflevector(lo, hi, 0, 1, 2, 3, 4, 5, 6, 7));
}
DEVI bf16x8 lds_frag(const char* a) { return *(const LAS bf16x8*)a; }
DEVI f32x4 mfma(bf16x8 a, bf16x8 b, f32x4 c) { return __builtin_amdgcn_mfma_f32_16x16x32_bf16(a, b, c, 0, 0, 0); }
DEVI unsigned pk(float a, float b) { f32x2 v = {a, b}; return __builtin_bit_cast(unsigned, __builtin_convertvector(v, bf16x2)); }
DEVI bf16x8 mk_frag(unsigned a, unsigned b, unsigned c, unsigned d) { u32x4 v = {a, b, c, d}; return __builtin_bit_cast(bf16x8, v); }
DEVI float lo_f(unsigned u) { return __uint_as_float(u << 16); }
DEVI float hi_f(unsigned u) { return __uint_as_float(u & 0xffff0000u); }
DEVI float max2(float a, float b) { return __builtin_elementwise_maximum(a, b); }
DEVI void swap32(float& a, float& b) { auto r = __builtin_amdgcn_permlane32_swap(__float_as_uint(a), __float_as_uint(b), false, false); a = __uint_as_float(r[0]); b = __uint_as_float(r[1]); }
DEVI void swap16(float& a, float& b) { auto r = __builtin_amdgcn_permlane16_swap(__float_as_uint(a), __float_as_uint(b), false, false); a = __uint_as_float(r[0]); b = __uint_as_float(r[1]); }
template <bool MAX> DEVI float red4(float a, float b, float c, float d) {
  swap32(a, b); swap32(c, d);
  float u = MAX ? max2(a, b) : a + b, v = MAX ? max2(c, d) : c + d;
  swap16(u, v);
  return MAX ? max2(u, v) : u + v;
}
DEVI void bcast4(float r, float& a, float& b, float& c, float& d) {
  float u = r, v = r;
  swap16(u, v);
  a = u; b = u; swap32(a, b);
  c = v; d = v; swap32(c, d);
}
template <int NM, int ND, int NV = 0> DEVI void weave() {
#pragma unroll
  for (int i = 0; i < NM; ++i) {
    __builtin_amdgcn_sched_group_barrier(0x008, 1, 0);
    __builtin_amdgcn_sched_group_barrier(0x100, ND, 0);
    if (NV) __builtin_amdgcn_sched_group_barrier(0x002, NV, 0);
  }
}
DEVI float dot2(unsigned a, unsigned b, float c) { return __builtin_amdgcn_fdot2_f32_bf16(__builtin_bit_cast(bf16x2, a), __builtin_bit_cast(bf16x2, b), c, false); }

constexpr unsigned SPIN_CAP = 1u << 17;
DEVI unsigned umin(unsigned a, unsigned b) { return a < b ? a : b; }
DEVI u32x4 lds_ld4(char* sm, int off) { return *(volatile LAS u32x4*)(sm + off); }
DEVI unsigned min4(u32x4 v) { return __builtin_amdgcn_readfirstlane(umin(umin(v[0], v[1]), umin(v[2], v[3]))); }
DEVI void lds_set(char* sm, int off, unsigned v) {
  asm volatile("s_mov_b64 exec, 1\n\tds_write_b32 %0, %1\n\ts_mov_b64 exec, -1" ::"v"(lds_addr(sm) + off), "v"(v) : "memory");
}
template <int NW> DEVI unsigned lds_wait(char* sm, int off, unsigned need) {
  unsigned m = 0;
#pragma unroll 1
  for (unsigned spin = 0; spin < SPIN_CAP; ++spin) {
    if (NW == 1) m = __builtin_amdgcn_readfirstlane(*(volatile LAS unsigned*)(sm + off));
    else if (NW == 4) m = min4(lds_ld4(sm, off));
    else { const u32x4 a = lds_ld4(sm, off), b = lds_ld4(sm, off + 16); m = min4((u32x4){umin(a[0], b[0]), umin(a[1], b[1]), umin(a[2], b[2]), umin(a[3], b[3])}); }
    if (m >= need) break;
    __builtin_amdgcn_s_sleep(1);
  }
  asm volatile("" ::: "memory");
  return m;
}
DEVI u64 next_tag(u64 e) { return e * 6364136223846793005ull + 1442695040888963407ull; }
DEVI u64 ld_agent(const char* p) { return __hip_atomic_load((const GAS u64*)p, __ATOMIC_RELAXED, __HIP_MEMORY_SCOPE_AGENT); }
DEVI void st_agent(char* p, u64 v) { __hip_atomic_store((GAS u64*)p, v, __ATOMIC_RELAXED, __HIP_MEMORY_SCOPE_AGENT); }
DEVI u64 get_tag(char* sm) { return next_tag(*(volatile LAS u64*)(sm + L_EP)); }
DEVI void poll_flags(const char* flag, u64 tag) {
#pragma unroll 1
  for (unsigned spin = 0; spin < SPIN_CAP; ++spin) {
    if (__all(ld_agent(flag) == tag)) break;
    __builtin_amdgcn_s_sleep(1);
  }
  asm volatile("" ::: "memory");
}
DEVI void publish(char* sm, int k, unsigned nwaves, char* flag) {
  wait_vm<0>();
  if ((threadIdx.x & 63) == 0) {
    const unsigned old = __hip_atomic_fetch_add((LAS unsigned*)(sm + L_PUB + k * 4), 1u, __ATOMIC_RELAXED, __HIP_MEMORY_SCOPE_WORKGROUP);
    if (old == nwaves - 1) st_agent(flag, get_tag(sm));
  }
}

DEVI void dual_exp(const float (&p)[2][5], const float sc, float (&e)[2][10], float& s) {
  float mx[2], mn[2];
#pragma unroll
  for (int h = 0; h < 2; ++h) {
    mx[h] = max2(max2(max2(max2(p[h][0], p[h][1]), p[h][2]), p[h][3]), p[h][4]);
    mn[h] = max2(max2(max2(max2(-p[h][0], -p[h][1]), -p[h][2]), -p[h][3]), -p[h][4]);
  }
  const float m = -sc * red4<true>(mx[0], mn[0], mx[1], mn[1]);
  float c[4];
  bcast4(m, c[0], c[1], c[2], c[3]);
  float sp[2], sn[2];
#pragma unroll
  for (int h = 0; h < 2; ++h) {
#pragma unroll
    for (int i = 0; i < 5; ++i) {
      e[h][i] = __builtin_amdgcn_exp2f(fmaf(p[h][i], sc, c[2 * h]));
      e[h][5 + i] = __builtin_amdgcn_exp2f(fmaf(p[h][i], -sc, c[2 * h + 1]));
    }
    sp[h] = (e[h][0] + e[h][1]) + (e[h][2] + e[h][3]) + e[h][4];
    sn[h] = (e[h][5] + e[h][6]) + (e[h][7] + e[h][8]) + e[h][9];
  }
  s = red4<false>(sp[0], sn[0], sp[1], sn[1]);
}

struct Loader {
  const Params& P; char* sm; const Tiles& T; int lw, l; unsigned sb;
  u32x4 rws;
  u32x4 sA, sB;
  unsigned voff, offA, offB, step, dst;
  int left, seg;

  DEVI void next_seg() {
    const int i = 8 * lw + (l >> 3), hj = i >> 4, gg = (i >> 2) & 3, e = i & 3;
    const unsigned sw = ((l & 7) ^ (((i >> 1) & 3) << 1)) * 16;
    ++seg;
    if (seg < 2 && !T.hasB) seg = 2;
    if (seg == 2 && !T.hasC) seg = 3;
    if (seg == 3 && !T.hasB) seg = 4;
    offA = 0;
    if (seg == 0) { sA = sB = mk_srd(P.wo, D * D * 2); voff = (20 * hj + 4 * gg + e) * 1280 + T.ctB * 128 + sw; offB = 40 * 1280; step = 80 * 1280; left = 8; }
    else if (seg == 1) { voff = (40 * gg + 20 * hj + 16 + e) * 1280 + T.ctB * 128 + sw; offB = 160 * 1280; step = 320 * 1280; left = 2; }
    else if (seg == 2) { sA = mk_srd(P.wg, D * DF * 2); sB = mk_srd(P.wu, D * DF * 2); voff = i * 3456 + T.ctC * 128 + sw; offB = 0; step = 32 * 3456; left = 20; }
    else if (seg == 3) { sA = sB = mk_srd(P.wd, DF * D * 2); voff = i * 1280 + T.ctB * 128 + sw; offB = 32 * 1280; step = 64 * 1280; left = 27; }
    else left = 0;
  }
  DEVI void issue_w() {
    if (left == 0) return;
    dma16(sA, dst, voff, offA);
    dma16(sB, dst + 4096, voff, offB);
    offA += step; offB += step;
    dst = dst + 8192 >= sb + MISC ? dst - (WR_N - 1) * 8192 : dst + 8192;
    if (--left == 0) next_seg();
  }
  DEVI void hop(const char* flag, unsigned id) {
    if (lw == 0) { poll_flags(flag, get_tag(sm)); lds_set(sm, L_GO, id); }
    else lds_wait<1>(sm, L_GO, id);
  }
  template <int NS, int R, int SLOT, int NA, class F, class G>
  DEVI void stream(F&& pieces, G&& after_last) {
    constexpr int PD = R - 1;
#pragma unroll
    for (int s = 0; s < PD; ++s) pieces(s, sb + AR + s * SLOT);
    wait_bar<(PD - 1) * NA>();
    pieces(PD, sb + AR + PD * SLOT);
    unsigned st = sb + AR;
#pragma unroll 1
    for (int s = 1; s + PD < NS; ++s) {
      wait_bar<(PD - 1) * NA>();
      issue_w();
      pieces(s + PD, st);
      st = st + SLOT == sb + AR + R * SLOT ? sb + AR : st + SLOT;
    }
    if constexpr (PD == 4) wait_bar<3 * NA>();
    wait_bar<2 * NA>();
    wait_bar<NA>();
    wait_bar<0>();
    after_last();
#pragma unroll 1
    for (int i = 0; i < PD; ++i) issue_w();
    __builtin_amdgcn_s_barrier();
    issue_w();
  }
};

DEVI void loader_main(const Params& P, char* sm, const Tiles& T, const int lw) {
  const int l = threadIdx.x & 63;
  const int t = l & 15, g = l >> 4, q = (l & 15) >> 2, pp = l & 3;
  const int p = T.p, j = T.j;
  const unsigned sb = lds_addr(sm);
  const u32x4 rx = mk_srd(P.x, S * D * 2), rw = mk_srd(P.wqkv, D * 3 * D * 2);
  const int rot = ((2 * (j & 7) + (j >> 3) + 4 * (p & 3) + (p >> 2)) & 15) * 10 / 16 * ROTA;
  unsigned xoff[2], woff[3], qoff[3];
#pragma unroll
  for (int i = 0; i < 2; ++i) xoff[i] = (p * 128 + 32 * lw + 8 * i + (l >> 3)) * 1280 + (((l & 7) ^ ((4 * i + (l >> 4)) & 7)) * 16);
#pragma unroll
  for (int i = 0; i < 3; ++i) {
    const int e = i * 64 + l, sl = e / 10, ch = e % 10;
    const int r = 16 * lw + ((sl & 3) | ((sl & 4) << 1) | ((sl & 8) >> 1));
    woff[i] = r * 3840 + (ch < 5 ? 1280 + 16 * ch : 2560 + 16 * (ch - 5)) + 80 * j;
  }
  auto issue = [&](int s) {
    const unsigned st = sb + ((s + 2) & 3) * A_ST;
    const int ks = s + rot >= 10 ? s + rot - 10 : s + rot;
    dma16(rw, st + 16384 + lw * 2560, woff[0], ks * 64 * 3840);
    dma16(rx, st + lw * 4096, xoff[0], ks * 128);
    dma16(rx, st + lw * 4096 + 1024, xoff[1], ks * 128);
    dma16(rw, st + 16384 + lw * 2560 + 1024, woff[1], ks * 64 * 3840);
    dma16(rx, st + lw * 4096 + 2048, xoff[0], ks * 128 + 16 * 1280);
    dma16(rx, st + lw * 4096 + 3072, xoff[1], ks * 128 + 16 * 1280);
    dma16m(rw, st + 16384 + lw * 2560 + 2048, woff[2], ks * 64 * 3840, 0xffffffffull);
  };
  __builtin_amdgcn_s_setprio(3);
  Loader L{P, sm, T, lw, l, sb};
  L.rws = mk_srd(P.ws, WS_END);
  issue(0);
  if (lw < 2) dma16m(mk_srd(P.wphi, 32 * 400 * 2), sb + L_PHI + lw * 1024, lw * 1024 + l * 16, j * 1600, lw == 0 ? ~0ull : (1ull << 36) - 1);
  else dma16m<SC1>(L.rws, sb + L_EP, l * 16, WS_EPOCH, 1ull);
  issue(1); issue(2);
#pragma unroll 1
  for (int s = 0; s < 7; ++s) {
    wait_bar<14>();
    issue(s + 3);
  }
#pragma unroll
  for (int i = 0; i < 3; ++i) {
    const int e = i * 64 + l, sl = e / 5, ch = e % 5, s16 = sl & 15;
    const int r = (sl & 16) | (s16 & 8) | ((s16 & 1) << 2) | ((s16 >> 1) & 3);
    qoff[i] = r * 3840 + 16 * ch + 80 * j;
  }
  auto issue_q = [&](int gq) {
    const unsigned st = sb + A_WQ + gq * 10240 + lw * 2560;
    int r0 = gq * 128 + lw * 32 + rot * 64;
    if (r0 >= 640) r0 -= 640;
    dma16(rw, st, qoff[0], r0 * 3840);
    dma16(rw, st + 1024, qoff[1], r0 * 3840);
    dma16m(rw, st + 2048, qoff[2], r0 * 3840, 0xffffffffull);
  };
  wait_bar<14>();
  issue_q(0); issue_q(1);
  wait_bar<7 + 6>();
  issue_q(2);
  wait_bar<9>();
  issue_q(3); issue_q(4);
  {
    const int hh = lw >> 1, ob = lw & 1;
    unsigned v[4];
#pragma unroll
    for (int jj = 0; jj < 8; ++jj) {
      const int d = 16 * (hh + (jj >> 2)) + 4 * g + (jj & 3) - 20 * hh;
      const int f = ob == 0 ? t : 16 + (t >> 2);
      const bool ok = d >= 0 && d < 20 && (ob == 0 || (t & 3) == 0);
      const unsigned e = *(const LAS u16*)(sm + L_PHI + hh * 800 + (ok ? d * 40 + f * 2 : 0));
      const unsigned m = ok ? e : 0u;
      if (jj & 1) v[jj >> 1] |= m << 16; else v[jj >> 1] = m;
    }
    *(LAS u32x4*)(sm + L_PHF + lw * 1024 + l * 16) = (u32x4){v[0], v[1], v[2], v[3]};
    wait_lds();
    lds_set(sm, L_SIG + lw * 4, 1);
  }
  L.seg = -1; L.dst = sb + WR + 6 * 8192 + lw * 1024;
  L.next_seg();
  wait_vm<0>();
  lds_set(sm, L_SIG + lw * 4, 2);
#pragma unroll 1
  for (int i = 0; i < 5; ++i) L.issue_w();
  lds_wait<4>(sm, L_SIG, 2);
  if (lw < 3) {
    const bf16x8 ones = mk_frag(0x3F803F80u, 0x3F803F80u, 0x3F803F80u, 0x3F803F80u);
    const char* qrd = sm + A_WQ + (16 * (g >> 1) + 2 * (4 * (g & 1) + q)) * 80 + pp * 8 + lw * 32;
    f32x4 cq = {0, 0, 0, 0};
#pragma unroll
    for (int k = 0; k < 20; ++k) cq = mfma(tr2(qrd + k * 2560, qrd + k * 2560 + 80), ones, cq);
    if (t == 0) *(LAS f32x4*)(sm + L_CS + 320 + lw * 64 + g * 16) = cq;
  } else {
    *(LAS u32x4*)(sm + L_ONE + l * 16) = (u32x4){0x3F803F80u, 0x3F803F80u, 0x3F803F80u, 0x3F803F80u};
  }
  wait_lds();
  lds_set(sm, L_SIG + lw * 4, 3);
  lds_wait<4>(sm, L_BAR, 3);
#pragma unroll 1
  for (int i = 0; i < 6; ++i) L.issue_w();
  STAMPT(512, 1, 8);
  L.hop(P.ws + WS_FKV + (j * 16 + (l & 15)) * 128, 1);
  STAMPT(512, 1, 9);
  {
    const rsrc_t rws = mk_rsrc(P.ws, WS_END);
    const int item = lw * 64 + l;
    if (item < 210) {
      u32x4 part[16];
#pragma unroll
      for (int i = 0; i < 16; ++i) part[i] = __builtin_amdgcn_raw_buffer_load_b128(rws, item * 16, WS_KVP + (j * 16 + i) * KVP_SZ, SC1);
      float sum[8];
      unsigned sel_lo = 0x00003F80u, sel_hi = 0x3F800000u;
      asm volatile("" : "+v"(sel_lo), "+v"(sel_hi));
#pragma unroll
      for (int c = 0; c < 4; ++c) { sum[2 * c] = lo_f(part[0][c]); sum[2 * c + 1] = hi_f(part[0][c]); }
#pragma unroll
      for (int i = 1; i < 16; ++i)
#pragma unroll
        for (int c = 0; c < 4; ++c) { sum[2 * c] = dot2(part[i][c], sel_lo, sum[2 * c]); sum[2 * c + 1] = dot2(part[i][c], sel_hi, sum[2 * c + 1]); }
      *(LAS u32x4*)(sm + L_KVS + item * 16) = (u32x4){pk(sum[0], sum[1]), pk(sum[2], sum[3]), pk(sum[4], sum[5]), pk(sum[6], sum[7])};
    }
    wait_lds();
    lds_set(sm, L_SIG + lw * 4, 4);
  }
  STAMPT(512, 1, 10);
  lds_wait<8>(sm, L_BAR, 4);
  if (T.hasB) {
#pragma unroll
    for (int n = 0; n < 3; ++n)
      dma16(rx, sb + AR + 4 * 12288 + (lw + 4 * n) * 1024, (l >> 3) * 1280 + (((l & 7) ^ (l >> 3)) * 16), (T.tbB * 16 + (lw + 4 * n) * 8) * 1280 + T.ctB * 128);
    wait_vm<0>();
    lds_set(sm, L_SIG + lw * 4, 5);
    const int p0 = T.tbB >> 3, p1 = (T.tbB + T.nbB - 1) >> 3;
    L.hop(P.ws + WS_FO + (((l & 16) ? p1 : p0) * 16 + (l & 15)) * 128, 2);
    STAMPT(512, 1, 11);
    if (blockIdx.x == 0 && lw == 0 && l == 0) st_agent(P.ws + WS_EPOCH, get_tag(sm));
    const bool three = lw + 8 < 2 * T.nbB;
    const unsigned aoff = WS_O + (T.tbB + (lw >> 1)) * 20 * 1024 + (lw & 1) * 1024;
    auto p2 = [&](int s, unsigned st) {
      dma16<SC1>(L.rws, st + lw * 1024, l * 16, aoff + s * 2048);
      dma16<SC1>(L.rws, st + (lw + 4) * 1024, l * 16, aoff + 2 * 20 * 1024 + s * 2048);
    };
    auto p3 = [&](int s, unsigned st) { p2(s, st); dma16<SC1>(L.rws, st + (lw + 8) * 1024, l * 16, aoff + 4 * 20 * 1024 + s * 2048); };
    if (three) L.stream<10, 5, 12288, 3>(p3, [&]() {}); else L.stream<10, 5, 12288, 2>(p2, [&]() {});
  }
  if (T.hasC) {
    const int r0 = ((T.tbC + 1) * 25 - 1) / 128, r1 = ((T.tbC + T.nbC) * 25 - 1) / 128;
    const int fr = r0 + l / 10;
    STAMPT(512, 2, 8);
    L.hop(P.ws + WS_FH + ((fr < r1 ? fr : r1) * 10 + l % 10) * 128, 3);
    STAMPT(512, 2, 9);
    const bool four = lw + 12 < T.nbC;
    const unsigned aoff = WS_H + (T.tbC + lw) * 20 * 1024;
    auto p3 = [&](int s, unsigned st) {
      dma16<SC1>(L.rws, st + lw * 1024, l * 16, aoff + s * 1024);
      dma16<SC1>(L.rws, st + (lw + 4) * 1024, l * 16, aoff + 4 * 20 * 1024 + s * 1024);
      dma16<SC1>(L.rws, st + (lw + 8) * 1024, l * 16, aoff + 8 * 20 * 1024 + s * 1024);
    };
    auto p4 = [&](int s, unsigned st) { p3(s, st); dma16<SC1>(L.rws, st + (lw + 12) * 1024, l * 16, aoff + 12 * 20 * 1024 + s * 1024); };
    auto stats = [&]() {
      const int tok = lw * 64 + l;
      if (tok < T.nbC * 16) {
        const rsrc_t rws = mk_rsrc(P.ws, WS_END);
        f32x2 sp[10];
#pragma unroll
        for (int c = 0; c < 10; ++c) sp[c] = __builtin_bit_cast(f32x2, __builtin_amdgcn_raw_buffer_load_b64(rws, tok * 8, WS_ST + (c * 2048 + T.tbC * 16) * 8, SC1));
        float s1 = 0.f, s2 = 0.f;
#pragma unroll
        for (int c = 0; c < 10; ++c) { s1 += sp[c][0]; s2 += sp[c][1]; }
        const float mean = s1 * (1.f / D);
        const float rstd = __builtin_amdgcn_rsqf(max2(s2 * (1.f / D) - mean * mean, 0.f) + 1e-5f);
        *(LAS f32x2*)(sm + L_STAT + tok * 8) = (f32x2){mean, rstd};
      }
      wait_lds();
      lds_set(sm, L_SIG + lw * 4, 6);
    };
    if (four) L.stream<20, 4, 15360, 4>(p4, stats); else L.stream<20, 4, 15360, 3>(p3, stats);
  }
  if (T.hasB) {
    const int r0 = ((T.tbB + 1) * 9 - 1) / 128, r1 = ((T.tbB + T.nbB) * 9 - 1) / 128;
    STAMPT(512, 3, 8);
    L.hop(P.ws + WS_FA + ((l < 27 ? r0 : r1) * 27 + l % 27) * 128, 4);
    STAMPT(512, 3, 9);
    const bool three = lw + 8 < 2 * T.nbB;
    const unsigned aoff = WS_ACT + (T.tbB + (lw >> 1)) * 54 * 1024 + (lw & 1) * 1024;
    auto p2 = [&](int s, unsigned st) {
      dma16<SC1>(L.rws, st + lw * 1024, l * 16, aoff + s * 2048);
      dma16<SC1>(L.rws, st + (lw + 4) * 1024, l * 16, aoff + 2 * 54 * 1024 + s * 2048);
    };
    auto p3 = [&](int s, unsigned st) { p2(s, st); dma16<SC1>(L.rws, st + (lw + 8) * 1024, l * 16, aoff + 4 * 54 * 1024 + s * 2048); };
    if (three) L.stream<27, 5, 12288, 3>(p3, [&]() {}); else L.stream<27, 5, 12288, 2>(p2, [&]() {});
  }
}

template <int N> DEVI void idle_bars() {
#pragma unroll 1
  for (int s = 0; s < N; ++s) __builtin_amdgcn_s_barrier();
}

DEVI void a_comp(const Params& P, char* sm, const Tiles& T, const int w) {
  const int l = threadIdx.x & 63, p = T.p, j = T.j;
  const int t = l & 15, g = l >> 4, q = (l & 15) >> 2, pp = l & 3;
  const rsrc_t rws = mk_rsrc(P.ws, WS_END);
  const bf16x8 ones = mk_frag(0x3F803F80u, 0x3F803F80u, 0x3F803F80u, 0x3F803F80u);
  const int xrd = w * 2048 + t * 128;
  const int xsw = (t >> 1) & 7;
  const int wrd = 16384 + (16 * (g >> 1) + 4 * (g & 1) + q) * 160 + pp * 8;
  const int wcs = wrd + (w < 4 ? w : 4) * 32;
  bf16x8 xf[20];
  f32x4 acc[5], accC = {0, 0, 0, 0};
  float s1 = 0.f, s2 = 0.f;
#pragma unroll
  for (int b = 0; b < 5; ++b) acc[b] = (f32x4){0, 0, 0, 0};
  struct WF { bf16x8 w[6]; };
  auto rd = [&](WF& f, int kc) {
    const char* st = sm + (((kc >> 1) + 2) & 3) * A_ST;
    const int k = kc & 1;
    xf[kc] = lds_frag(st + xrd + (((4 * k + g) ^ xsw) * 16));
#pragma unroll
    for (int b = 0; b < 5; ++b) f.w[b] = tr2(st + wrd + k * 5120 + b * 32, st + wrd + k * 5120 + 1280 + b * 32);
    f.w[5] = tr2(st + wcs + k * 5120, st + wcs + k * 5120 + 1280);
  };
  auto mm = [&](const WF& f, int kc) {
#pragma unroll
    for (int b = 0; b < 5; ++b) acc[b] = mfma(f.w[b], xf[kc], acc[b]);
    accC = mfma(f.w[5], ones, accC);
    const u32x4 xv = __builtin_bit_cast(u32x4, xf[kc]);
#pragma unroll
    for (int i = 0; i < 4; ++i) { s1 = dot2(xv[i], 0x3F803F80u, s1); s2 = dot2(xv[i], xv[i], s2); }
  };
  STAMP(0, 0);
  WF f0, f1;
#pragma unroll
  for (int s = 0; s < 10; ++s) {
    lds_bar();
    STAMP(0, 1 + s);
    rd(f0, 2 * s);
    if (s > 0) { mm(f1, 2 * s - 1); weave<6, 2, 1>(); asm volatile("" : "+v"(s1), "+v"(s2)); }
    __builtin_amdgcn_sched_barrier(0);
    rd(f1, 2 * s + 1);
    mm(f0, 2 * s);
    weave<6, 2, 1>();
    asm volatile("" : "+v"(s1), "+v"(s2));
    __builtin_amdgcn_sched_barrier(0);
  }
  mm(f1, 19);
  STAMP(0, 11);
  if (t == 0 && w < 5) *(LAS f32x4*)(sm + L_CS + w * 64 + g * 16) = accC;
  wait_lds();
  lds_set(sm, L_BAR + w * 4, 1);
  swap32(s1, s2);
  float su = s1 + s2, sv = su;
  swap16(su, sv);
  s1 = su + sv; s2 = s1;
  swap32(s1, s2);
  const float mean = s1 * (1.f / D);
  const float rstd = __builtin_amdgcn_rsqf(max2(s2 * (1.f / D) - mean * mean, 0.f) + 1e-5f);
  const float sc = rstd * 1.4426950408889634f;
  lds_wait<4>(sm, L_SIG, 1);
  bf16x8 phiA[2][2];
#pragma unroll
  for (int i = 0; i < 4; ++i) phiA[i >> 1][i & 1] = lds_frag(sm + L_PHF + i * 1024 + l * 16);
  lds_wait<8>(sm, L_BAR, 1);

  {
    float v[5][4];
#pragma unroll
    for (int b = 0; b < 5; ++b) {
      const f32x4 cs = *(const LAS f32x4*)(sm + L_CS + b * 64 + g * 16);
#pragma unroll
      for (int r = 0; r < 4; ++r) v[b][r] = fmaf(-mean, cs[r], acc[b][r]);
    }
    unsigned kd[3][2];
#pragma unroll
    for (int b = 0; b < 3; ++b) { kd[b][0] = pk(v[b][0], v[b][1]); kd[b][1] = pk(v[b][2], v[b][3]); }
    float pa[2][5];
#pragma unroll
    for (int hh = 0; hh < 2; ++hh) {
      const bf16x8 kb = mk_frag(kd[hh][0], kd[hh][1], kd[hh + 1][0], kd[hh + 1][1]);
      const f32x4 z = {0, 0, 0, 0};
      const f32x4 p0 = mfma(phiA[hh][0], kb, z), p1 = mfma(phiA[hh][1], kb, z);
      pa[hh][0] = p0[0]; pa[hh][1] = p0[1]; pa[hh][2] = p0[2]; pa[hh][3] = p0[3]; pa[hh][4] = p1[0];
    }
    char* row = sm + A_TOK + (16 * w + t) * 288;
    {
      const u32x2 v2 = {pk(v[2][0] * rstd, v[2][1] * rstd), pk(v[2][2] * rstd, v[2][3] * rstd)};
      const u32x2 one = {0x3F80u, 0u};
      *(LAS u32x2*)(row + (g >= 2 ? 176 : 272) + g * 8) = g >= 2 ? v2 : one;
      *(LAS u32x2*)(row + 208 + g * 8) = (u32x2){pk(v[3][0] * rstd, v[3][1] * rstd), pk(v[3][2] * rstd, v[3][3] * rstd)};
      *(LAS u32x2*)(row + 240 + g * 8) = (u32x2){pk(v[4][0] * rstd, v[4][1] * rstd), pk(v[4][2] * rstd, v[4][3] * rstd)};
    }
    float e[2][10], s, r[4];
    dual_exp(pa, sc, e, s);
    bcast4(__builtin_amdgcn_rcpf(s), r[0], r[1], r[2], r[3]);
#pragma unroll
    for (int hh = 0; hh < 2; ++hh) {
      const float rp = r[2 * hh], rn = r[2 * hh + 1];
      *(LAS u32x2*)(row + hh * 96 + g * 8) = (u32x2){pk(e[hh][0] * rp, e[hh][1] * rp), pk(e[hh][2] * rp, e[hh][3] * rp)};
      *(LAS u32x2*)(row + hh * 96 + 32 + g * 8) = (u32x2){pk(e[hh][4] * rp, e[hh][5] * rn), pk(e[hh][6] * rn, e[hh][7] * rn)};
      *(LAS u32x2*)(row + hh * 96 + 64 + g * 8) = (u32x2){pk(e[hh][8] * rn, e[hh][9] * rn), 0u};
    }
  }
  wait_lds();
  lds_set(sm, L_BAR + w * 4, 2);
  STAMP(0, 12);

  if (w < 4) {
    lds_wait<8>(sm, L_BAR, 2);
    const int hh = w >> 1, db = w & 1;
    const char* base = sm + A_TOK + (4 * g + q) * 288;
    const char* pa = base + hh * 96 + pp * 8;
    const int vg = db == 0 ? 5 * hh + pp : (pp == 0 ? 5 * hh + 4 : 10);
    const char* pb = base + 192 + vg * 8;
    f32x4 kv[3] = {{0, 0, 0, 0}, {0, 0, 0, 0}, {0, 0, 0, 0}};
#pragma unroll
    for (int k = 0; k < 4; ++k) {
      const bf16x8 vb = tr2(pb + k * 32 * 288, pb + (k * 32 + 16) * 288);
#pragma unroll
      for (int b = 0; b < 3; ++b) kv[b] = mfma(tr2(pa + k * 32 * 288 + b * 32, pa + (k * 32 + 16) * 288 + b * 32), vb, kv[b]);
    }
    const unsigned dst = WS_KVP + (j * 16 + p) * KVP_SZ;
    const int item = hh * 84 + (db == 0 ? l : 64 + g * 5 + t);
    if (db == 0 || t < 5) {
      const u32x4 m = {pk(kv[0][0], kv[0][1]), pk(kv[0][2], kv[0][3]), pk(kv[1][0], kv[1][1]), pk(kv[1][2], kv[1][3])};
      __builtin_amdgcn_raw_buffer_store_b128(m, rws, item * 16, dst, SC1);
      __builtin_amdgcn_raw_buffer_store_b32(pk(kv[2][0], kv[2][1]), rws, 2688 + item * 4, dst, SC1);
    }
    wait_lds();
    lds_set(sm, L_BAR + w * 4, 3);
  }
  f32x4 aq[3] = {{0, 0, 0, 0}, {0, 0, 0, 0}, {0, 0, 0, 0}};
  lds_wait<4>(sm, L_SIG, 2);
  {
    const char* qrd = sm + A_WQ + (16 * (g >> 1) + 2 * (4 * (g & 1) + q)) * 80 + pp * 8;
    bf16x8 qa[2][6];
    auto rq = [&](int i, int k2) {
#pragma unroll
      for (int u = 0; u < 6; ++u) qa[i][u] = tr2(qrd + (2 * k2 + u / 3) * 2560 + (u % 3) * 32, qrd + (2 * k2 + u / 3) * 2560 + 80 + (u % 3) * 32);
    };
    rq(0, 0);
#pragma unroll
    for (int k2 = 0; k2 < 10; ++k2) {
      if (k2 == 3) { if (w < 4) publish(sm, 0, 4, P.ws + WS_FKV + (j * 16 + p) * 128); STAMP(0, 13); }
      if (k2 < 9) rq((k2 + 1) & 1, k2 + 1);
#pragma unroll
      for (int u = 0; u < 6; ++u) aq[u % 3] = mfma(qa[k2 & 1][u], xf[2 * k2 + u / 3], aq[u % 3]);
      if (k2 < 9) weave<6, 2>();
      __builtin_amdgcn_sched_barrier(0);
    }
  }
  lds_wait<4>(sm, L_SIG, 3);
  wait_lds();
  lds_set(sm, L_BAR + w * 4, 4);
  STAMP(0, 14);
  unsigned fq[2][5];
  float spq[2];
  {
    unsigned qd[3][2];
#pragma unroll
    for (int b = 0; b < 3; ++b) {
      const f32x4 cs = *(const LAS f32x4*)(sm + L_CS + 320 + b * 64 + g * 16);
      float v[4];
#pragma unroll
      for (int r = 0; r < 4; ++r) v[r] = fmaf(-mean, cs[r], aq[b][r]);
      qd[b][0] = pk(v[0], v[1]); qd[b][1] = pk(v[2], v[3]);
    }
    if (g >= 2) { qd[2][0] = 0u; qd[2][1] = 0u; }
    float pa[2][5];
#pragma unroll
    for (int hh = 0; hh < 2; ++hh) {
      const bf16x8 qb = mk_frag(qd[hh][0], qd[hh][1], qd[hh + 1][0], qd[hh + 1][1]);
      const f32x4 z = {0, 0, 0, 0};
      const f32x4 p0 = mfma(phiA[hh][0], qb, z), p1 = mfma(phiA[hh][1], qb, z);
      pa[hh][0] = p0[0]; pa[hh][1] = p0[1]; pa[hh][2] = p0[2]; pa[hh][3] = p0[3]; pa[hh][4] = p1[0];
    }
    float e[2][10], s, sv4[4];
    dual_exp(pa, sc, e, s);
    bcast4(s, sv4[0], sv4[1], sv4[2], sv4[3]);
#pragma unroll
    for (int hh = 0; hh < 2; ++hh) {
      const float rn = sv4[2 * hh] * __builtin_amdgcn_rcpf(sv4[2 * hh + 1]);
      spq[hh] = sv4[2 * hh];
      fq[hh][0] = pk(e[hh][0], e[hh][1]); fq[hh][1] = pk(e[hh][2], e[hh][3]);
      fq[hh][2] = pk(e[hh][4], e[hh][5] * rn); fq[hh][3] = pk(e[hh][6] * rn, e[hh][7] * rn);
      fq[hh][4] = pk(e[hh][8] * rn, e[hh][9] * rn);
    }
    asm volatile("" : "+v"(fq[0][0]), "+v"(fq[0][1]), "+v"(fq[0][2]), "+v"(fq[0][3]), "+v"(fq[0][4]),
                      "+v"(fq[1][0]), "+v"(fq[1][1]), "+v"(fq[1][2]), "+v"(fq[1][3]), "+v"(fq[1][4]));
  }
  STAMP(0, 15);
  lds_wait<4>(sm, L_SIG, 4);
  STAMP(0, 16);

  {
    unsigned om[4], ol[4];
#pragma unroll
    for (int hh = 0; hh < 2; ++hh) {
      const bf16x8 b0 = mk_frag(fq[hh][0], fq[hh][1], fq[hh][2], fq[hh][3]);
      const bf16x8 b1 = mk_frag(fq[hh][4], 0u, 0u, 0u);
      f32x4 n[2];
#pragma unroll
      for (int db = 0; db < 2; ++db) {
        const int it = hh * 84 + (db == 0 ? l : 64 + g * 5 + (t < 5 ? t : 4));
        f32x4 c = {0, 0, 0, 0};
        c = mfma(lds_frag(sm + L_KVS + it * 16), b0, c);
        c = mfma(mk_frag(*(const LAS unsigned*)(sm + L_KVS + 2688 + it * 4), 0u, 0u, 0u), b1, c);
        n[db] = c;
      }
      float da = n[1][0], dbv = n[1][0];
      swap16(da, dbv);
      const float rd = __builtin_amdgcn_rcpf(fmaf(spq[hh], 1e-6f, dbv));
      om[2 * hh] = pk(n[0][0] * rd, n[0][1] * rd); om[2 * hh + 1] = pk(n[0][2] * rd, n[0][3] * rd);
      ol[2 * hh] = pk(n[1][0] * rd, n[1][1] * rd); ol[2 * hh + 1] = pk(n[1][2] * rd, n[1][3] * rd);
    }
    const unsigned tb = WS_O + (p * 8 + w) * 20 * 1024;
    __builtin_amdgcn_raw_buffer_store_b128((u32x4){om[0], om[1], om[2], om[3]}, rws, l * 16, tb + j * 1024, SC1);
    if (g == 0) __builtin_amdgcn_raw_buffer_store_b128((u32x4){ol[0], ol[1], ol[2], ol[3]}, rws, t * 16, tb + (16 + (j >> 2)) * 1024 + (j & 3) * 256, SC1);
  }
  STAMP(0, 17);
  publish(sm, 1, 8, P.ws + WS_FO + (p * 16 + j) * 128);
  STAMP(0, 18);
}

template <int NS>
DEVI void g_main(char* sm, int w, int wslot, f32x4 (&acc)[3][2]) {
  const int l = threadIdx.x & 63;
  const int g = l >> 4, q = (l & 15) >> 2, pp = l & 3, mh = w >> 1, nh = w & 1;
  const int ard = AR + mh * 6144 + l * 16;
  const int wrow = WR + (4 * g + q) * 128 + pp * 8, wsw = (2 * g + (q >> 1)) & 3;
  const int w0 = wrow + (((2 * nh) ^ wsw) * 32), w1 = wrow + (((2 * nh + 1) ^ wsw) * 32);
  struct Frags { bf16x8 a[2][3], w[2][2]; };
  int aslot = 0;
  auto rd = [&](Frags& f) {
    const char* sa = sm + aslot * 12288 + ard;
    const char* sw = sm + wslot * 8192;
#pragma unroll
    for (int k = 0; k < 2; ++k) {
      f.w[k][0] = tr2(sw + w0 + k * 4096, sw + w0 + k * 4096 + 2048);
      f.a[k][0] = lds_frag(sa + k * 1024);
      f.w[k][1] = tr2(sw + w1 + k * 4096, sw + w1 + k * 4096 + 2048);
      f.a[k][1] = lds_frag(sa + 2048 + k * 1024);
      f.a[k][2] = lds_frag(sa + 4096 + k * 1024);
    }
    aslot = aslot == 4 ? 0 : aslot + 1;
    wslot = wslot + 1 == WR_N ? 0 : wslot + 1;
  };
  auto mm = [&](const Frags& f) {
#pragma unroll
    for (int k = 0; k < 2; ++k)
#pragma unroll
      for (int m = 0; m < 3; ++m)
#pragma unroll
        for (int c = 0; c < 2; ++c) acc[m][c] = mfma(f.w[k][c], f.a[k][m], acc[m][c]);
  };
  auto step = [&](const Frags& cur, Frags& nxt) {
    lds_bar();
    rd(nxt);
    mm(cur);
    weave<7, 2>();
    __builtin_amdgcn_sched_barrier(0);
  };
  Frags f0, f1;
  lds_bar();
  rd(f0);
#pragma unroll 1
  for (int s = 0; s + 2 < NS; s += 2) { step(f0, f1); step(f1, f0); }
  if (NS % 2 == 0) { step(f0, f1); mm(f1); }
  else mm(f0);
}

DEVI void b_comp(const Params& P, char* sm, const Tiles& T, int w, unsigned (&hp)[3][4]) {
  if (w >= 4) { idle_bars<11>(); return; }
  const int l = threadIdx.x & 63, t = l & 15, g = l >> 4, mh = w >> 1, nh = w & 1;
  const rsrc_t rws = mk_rsrc(P.ws, WS_END);
  f32x4 acc[3][2];
  lds_wait<4>(sm, L_SIG, 5);
#pragma unroll
  for (int m = 0; m < 3; ++m)
#pragma unroll
    for (int c = 0; c < 2; ++c) {
      const u32x2 xr = *(const LAS u32x2*)(sm + AR + 4 * 12288 + (3 * mh + m) * 2048 + t * 128 + (((4 * nh + 2 * c + (g >> 1)) ^ (t & 7)) * 16) + (g & 1) * 8);
      acc[m][c] = (f32x4){lo_f(xr[0]), hi_f(xr[0]), lo_f(xr[1]), hi_f(xr[1])};
    }
  STAMP(1, 0);
  g_main<10>(sm, w, 6, acc);
  STAMP(1, 1);
  float st[3][2];
#pragma unroll
  for (int m = 0; m < 3; ++m) {
    const f32x4 a = acc[m][0], b = acc[m][1];
    hp[m][0] = pk(a[0], a[1]); hp[m][1] = pk(a[2], a[3]); hp[m][2] = pk(b[0], b[1]); hp[m][3] = pk(b[2], b[3]);
    if (3 * mh + m < T.nbB)
      __builtin_amdgcn_raw_buffer_store_b128((u32x4){hp[m][0], hp[m][1], hp[m][2], hp[m][3]}, rws, l * 16, WS_H + ((T.tbB + 3 * mh + m) * 20 + 2 * T.ctB + nh) * 1024, SC1);
    float s1 = 0.f, s2 = 0.f;
#pragma unroll
    for (int i = 0; i < 4; ++i) { s1 = dot2(hp[m][i], 0x3F803F80u, s1); s2 = dot2(hp[m][i], hp[m][i], s2); }
    st[m][0] = s1; st[m][1] = s2;
  }
  const float r01 = red4<false>(st[0][0], st[0][1], st[1][0], st[1][1]);
  swap32(st[2][0], st[2][1]);
  float u = st[2][0] + st[2][1], v = u;
  swap16(u, v);
  const float r2 = u + v;
  char* ex = sm + L_PHF + mh * 512;
  if (nh == 1) {
    *(LAS f32x2*)(ex + l * 8) = (f32x2){r01, r2};
    wait_lds();
    lds_set(sm, L_BAR + w * 4, 5);
  } else {
    lds_wait<1>(sm, L_BAR + (w + 1) * 4, 5);
    const f32x2 o = *(const LAS f32x2*)(ex + l * 8);
    const unsigned base = WS_ST + (T.ctB * 2048 + (T.tbB + 3 * mh) * 16) * 8;
    __builtin_amdgcn_raw_buffer_store_b32(__float_as_uint(r01 + o[0]), rws, ((g & 1) * 16 + t) * 8 + (g >> 1) * 4, base, SC1);
    if ((g & 1) == 0 && 3 * mh + 2 < T.nbB) __builtin_amdgcn_raw_buffer_store_b32(__float_as_uint(r2 + o[1]), rws, (32 + t) * 8 + (g >> 1) * 4, base, SC1);
  }
  STAMP(1, 2);
  publish(sm, 2, 4, P.ws + WS_FH + (T.rtB * 10 + T.ctB) * 128);
  STAMP(1, 3);
  __builtin_amdgcn_s_barrier();
}

DEVI void d_comp(const Params& P, char* sm, const Tiles& T, int w, const unsigned (&hp)[3][4]) {
  if (w >= 4) return;
  const int l = threadIdx.x & 63, t = l & 15, g = l >> 4, mh = w >> 1, nh = w & 1;
  const rsrc_t ro = mk_rsrc(P.out, S * D * 2);
  f32x4 acc[3][2];
#pragma unroll
  for (int m = 0; m < 3; ++m)
#pragma unroll
    for (int c = 0; c < 2; ++c) acc[m][c] = (f32x4){lo_f(hp[m][2 * c]), hi_f(hp[m][2 * c]), lo_f(hp[m][2 * c + 1]), hi_f(hp[m][2 * c + 1])};
  STAMP(3, 0);
  const int ws0 = (6 + 10 + (T.hasC ? 20 : 0)) % WR_N;
  g_main<27>(sm, w, ws0, acc);
  STAMP(3, 1);
  char* stg = sm + WR + ((ws0 + 27 + w) % WR_N) * 8192;
#pragma unroll
  for (int m = 0; m < 3; ++m)
#pragma unroll
    for (int c = 0; c < 2; ++c)
      *(LAS u32x2*)(stg + (m * 16 + t) * 80 + c * 32 + g * 8) = (u32x2){pk(acc[m][c][0], acc[m][c][1]), pk(acc[m][c][2], acc[m][c][3])};
  wait_lds();
#pragma unroll
  for (int m = 0; m < 3; ++m) {
    const u32x4 v = *(const LAS u32x4*)(stg + (m * 16 + (l >> 2)) * 80 + (l & 3) * 16);
    if (3 * mh + m < T.nbB)
      __builtin_amdgcn_raw_buffer_store_b128(v, ro, (l >> 2) * 1280 + (l & 3) * 16, (T.tbB + 3 * mh + m) * 16 * 1280 + T.ctB * 128 + nh * 64, 0);
  }
  STAMP(3, 2);
}

DEVI void c_comp(const Params& P, char* sm, const Tiles& T, int w) {
  const int l = threadIdx.x & 63;
  const int t = l & 15, g = l >> 4, q = (l & 15) >> 2, pp = l & 3, mq = w >> 1, nh = w & 1;
  const rsrc_t rws = mk_rsrc(P.ws, WS_END);
  STAMP(2, 0);
  const int ard = AR + mq * 4096 + l * 16;
  const int wrow = WR + (4 * g + q) * 128 + pp * 8, wsw = (2 * g + (q >> 1)) & 3;
  const int w0 = wrow + (((2 * nh) ^ wsw) * 32), w1 = wrow + (((2 * nh + 1) ^ wsw) * 32);
  f32x4 ag[4][2], au[4][2];
#pragma unroll
  for (int m = 0; m < 4; ++m)
#pragma unroll
    for (int c = 0; c < 2; ++c) { ag[m][c] = (f32x4){0, 0, 0, 0}; au[m][c] = (f32x4){0, 0, 0, 0}; }
  struct Frags { bf16x8 g[2], u[2], a[4]; };
  int aslot = 0, wslot = (6 + (T.hasB ? 10 : 0)) % WR_N;
  auto rd = [&](Frags& f) {
    const char* sa = sm + aslot * 15360 + ard;
    const char* sw = sm + wslot * 8192;
    f.g[0] = tr2(sw + w0, sw + w0 + 2048);
    f.a[0] = lds_frag(sa);
    f.g[1] = tr2(sw + w1, sw + w1 + 2048);
    f.a[1] = lds_frag(sa + 1024);
    f.u[0] = tr2(sw + w0 + 4096, sw + w0 + 6144);
    f.a[2] = lds_frag(sa + 2048);
    f.u[1] = tr2(sw + w1 + 4096, sw + w1 + 6144);
    f.a[3] = lds_frag(sm + (mq == 3 ? L_ONE : AR + aslot * 15360 + mq * 4096 + 3072) + l * 16);
    aslot = (aslot + 1) & 3;
    wslot = wslot + 1 == WR_N ? 0 : wslot + 1;
  };
  auto mm = [&](const Frags& f) {
#pragma unroll
    for (int m = 0; m < 4; ++m)
#pragma unroll
      for (int c = 0; c < 2; ++c) { ag[m][c] = mfma(f.g[c], f.a[m], ag[m][c]); au[m][c] = mfma(f.u[c], f.a[m], au[m][c]); }
  };
  auto step = [&](const Frags& cur, Frags& nxt) {
    lds_bar();
    rd(nxt);
    mm(cur);
    weave<12, 1>();
    __builtin_amdgcn_sched_barrier(0);
  };
  Frags f0, f1;
  lds_bar();
  STAMP(2, 1);
  rd(f0);
#pragma unroll 1
  for (int s = 0; s + 2 < 20; s += 2) { step(f0, f1); step(f1, f0); }
  step(f0, f1);
  mm(f1);
  STAMP(2, 2);
  if (mq == 3) {
    if (t == 0) {
#pragma unroll
      for (int c = 0; c < 2; ++c) {
        *(LAS f32x4*)(sm + L_CS + nh * 256 + c * 128 + g * 16) = ag[3][c];
        *(LAS f32x4*)(sm + L_CS + nh * 256 + c * 128 + 64 + g * 16) = au[3][c];
      }
    }
    wait_lds();
    lds_set(sm, L_BAR + w * 4, 6);
  }
  lds_wait<4>(sm, L_SIG, 6);
  lds_wait<1>(sm, L_BAR + (6 + nh) * 4, 6);
  f32x4 csg[2], csu[2];
#pragma unroll
  for (int c = 0; c < 2; ++c) {
    csg[c] = *(const LAS f32x4*)(sm + L_CS + nh * 256 + c * 128 + g * 16);
    csu[c] = *(const LAS f32x4*)(sm + L_CS + nh * 256 + c * 128 + 64 + g * 16);
  }
  constexpr float C0 = -2.f * 0.7978845608028654f * 1.4426950408889634f, C1 = C0 * 0.044715f;
#pragma unroll
  for (int m = 0; m < 4; ++m) {
    if (4 * mq + m < T.nbC) {
      const f32x2 st = *(const LAS f32x2*)(sm + L_STAT + ((4 * mq + m) * 16 + t) * 8);
      const float r = st[1], c0 = C0 * r, c1 = C1 * r * r * r, ir2 = __builtin_amdgcn_rcpf(r * r);
      unsigned o[4];
#pragma unroll
      for (int c = 0; c < 2; ++c) {
        float v[4];
#pragma unroll
        for (int i = 0; i < 4; ++i) {
          const float G = fmaf(-st[0], csg[c][i], ag[m][c][i]);
          const float U = fmaf(-st[0], csu[c][i], au[m][c][i]);
          const float ex = __builtin_amdgcn_exp2f(G * fmaf(G * G, c1, c0));
          v[i] = G * U * __builtin_amdgcn_rcpf(fmaf(ex, ir2, ir2));
        }
        o[2 * c] = pk(v[0], v[1]); o[2 * c + 1] = pk(v[2], v[3]);
      }
      __builtin_amdgcn_raw_buffer_store_b128((u32x4){o[0], o[1], o[2], o[3]}, rws, l * 16, WS_ACT + ((T.tbC + 4 * mq + m) * 54 + 2 * T.ctC + nh) * 1024, SC1);
    }
  }
  STAMP(2, 3);
  publish(sm, 3, 8, P.ws + WS_FA + (T.rtC * 27 + T.ctC) * 128);
  STAMP(2, 4);
  __builtin_amdgcn_s_barrier();
}

extern __shared__ __attribute__((aligned(16))) char smem[];

__global__ __launch_bounds__(NT) void k_block(Params P) {
  const int b = blockIdx.x, xcd = b & 7, idx = b >> 3;
  const int w = __builtin_amdgcn_readfirstlane(threadIdx.x >> 6);
  Tiles T;
  T.p = 4 * (xcd >> 1) + (idx >> 3); T.j = 8 * (xcd & 1) + (idx & 7);
  {
    const int qn = (xcd >> 1) * 32 + idx;
    T.hasB = qn < 125;
    T.rtB = qn / 5; T.ctB = 5 * (xcd & 1) + qn % 5;
    T.tbB = T.rtB * 128 / 25; T.nbB = (T.rtB + 1) * 128 / 25 - T.tbB;
  }
  {
    const int lo = xcd * 243 / 8, hi = (xcd + 1) * 243 / 8, qn = hi - 1 - idx;
    T.hasC = qn >= lo;
    const int qc = T.hasC ? qn : lo, band = qc / 81, rem = qc % 81;
    T.rtC = band * 3 + rem % 3; T.ctC = rem / 3;
    T.tbC = T.rtC * 128 / 9; T.nbC = (T.rtC + 1) * 128 / 9 - T.tbC;
  }
  if (w >= 8) { loader_main(P, smem, T, w - 8); return; }
  if (threadIdx.x < 32) *(LAS unsigned*)(smem + MISC + threadIdx.x * 4) = 0u;
  unsigned hp[3][4];
  a_comp(P, smem, T, w);
  if (T.hasB) b_comp(P, smem, T, w, hp);
  if (T.hasC) c_comp(P, smem, T, w);
  if (T.hasB) d_comp(P, smem, T, w, hp);
}

extern "C" void kernel_launch(void* const* d_in, const int* in_sizes, int n_in, void* d_out, int out_size, void* d_ws, size_t ws_size,
                              hipStream_t stream) {
  Params P;
  P.x = (const u16*)d_in[0]; P.wqkv = (const u16*)d_in[1]; P.wphi = (const u16*)d_in[2]; P.wo = (const u16*)d_in[3];
  P.wg = (const u16*)d_in[5]; P.wu = (const u16*)d_in[6]; P.wd = (const u16*)d_in[7];
  P.out = (u16*)d_out; P.ws = (char*)d_ws;
  (void)hipFuncSetAttribute((const void*)k_block, hipFuncAttributeMaxDynamicSharedMemorySize, LDS_BYTES);
  k_block<<<256, NT, LDS_BYTES, stream>>>(P);
}
```

```cpp
#include <hip/hip_runtime.h>
#include <type_traits>

typedef unsigned short u16;
typedef unsigned long long u64;
typedef __bf16 bf16x8 __attribute__((ext_vector_type(8)));
typedef __bf16 bf16x2 __attribute__((ext_vector_type(2)));
typedef short s16x4 __attribute__((ext_vector_type(4)));
typedef float f32x4 __attribute__((ext_vector_type(4)));
typedef float f32x2 __attribute__((ext_vector_type(2)));
typedef unsigned u32x4 __attribute__((ext_vector_type(4)));
typedef unsigned u32x2 __attribute__((ext_vector_type(2)));
typedef __amdgpu_buffer_rsrc_t rsrc_t;
#define LAS __attribute__((address_space(3)))
#define GAS __attribute__((address_space(1)))
#define DEVI __device__ __forceinline__

constexpr int S = 2048, D = 640, DF = 1728;
constexpr int NT = 768;
constexpr int SC1 = 16;
#define DUMPW 0
#define NLOOP 13
#define YPRE 1
#define ROTA 1

constexpr unsigned WS_EPOCH = 0;
constexpr unsigned WS_FKV = 4096;
constexpr unsigned WS_FO = WS_FKV + 32768;
constexpr unsigned WS_FH = WS_FO + 32768;
constexpr unsigned WS_FA = WS_FH + 32768;
constexpr unsigned WS_KVP = 139264;
constexpr unsigned KVP_SZ = 3456;
constexpr unsigned WS_O = 1u << 20;
constexpr unsigned WS_H = WS_O + 128 * 20 * 1024;
constexpr unsigned WS_ST = WS_H + 128 * 20 * 1024;
constexpr unsigned WS_ACT = WS_ST + 10 * 2048 * 8;
constexpr unsigned WS_END = WS_ACT + 128 * 54 * 1024;

constexpr int AR = 0;
constexpr int WR = 61440, WR_N = 11;
constexpr int MISC = WR + WR_N * 8192;
constexpr int L_BAR = MISC + 48;
constexpr int L_SIG = MISC + 80;
constexpr int L_GO = MISC + 96;
constexpr int L_PUB = MISC + 112;
constexpr int L_EP = MISC + 128;
constexpr int L_CS = MISC + 256;
constexpr int L_PHI = L_CS + 512;
constexpr int L_STAT = L_PHI + 2048;
constexpr int L_PHF = L_PHI + 2048;
constexpr int L_KVS = L_PHF + 4096;
constexpr int L_ONE = L_KVS + KVP_SZ;
constexpr int L_JUNK = L_ONE + 1024;
constexpr int LDS_BYTES = L_JUNK + 256;
static_assert(LDS_BYTES <= 163840, "LDS");
constexpr int A_ST = 26624;
constexpr int A_WQ = 0;
constexpr int A_TOK = WR + 6 * 8192;

#define STAMP(ph, i) do { } while (0)
#define DUMP(half) do { } while (0)
struct Dbg { };
#define STAMPT(T, ph, i) STAMP(ph, i)
#define LDS_TOTAL LDS_BYTES

struct Params {
  const u16* x; const u16* wqkv; const u16* wphi; const u16* wo;
  const u16* wg; const u16* wu; const u16* wd; u16* out; char* ws;
};
struct Tiles { int p, j; int hasB, ctB, tbB, nbB, rtB; int hasC, ctC, tbC, nbC, rtC; };

DEVI rsrc_t mk_rsrc(const void* p, unsigned bytes) { return __builtin_amdgcn_make_buffer_rsrc((void*)p, 0, bytes, 0x00020000); }
DEVI u32x4 mk_srd(const void* p, unsigned bytes) {
  const u64 a = (u64)p;
  return (u32x4){(unsigned)a, (unsigned)(a >> 32) & 0xffffu, bytes, 0x00020000u};
}
template <int AUX = 0> DEVI void dma16(u32x4 srd, unsigned lds, unsigned voff, unsigned soff) {
  if constexpr (AUX == 0)
    asm volatile("s_mov_b32 m0, %0\n\ts_nop 0\n\tbuffer_load_dwordx4 %1, %2, %3 offen lds" ::"s"(lds), "v"(voff), "s"(srd), "s"(soff) : "memory");
  else
    asm volatile("s_mov_b32 m0, %0\n\ts_nop 0\n\tbuffer_load_dwordx4 %1, %2, %3 offen sc1 lds" ::"s"(lds), "v"(voff), "s"(srd), "s"(soff) : "memory");
}
template <int AUX = 0> DEVI void dma16m(u32x4 srd, unsigned lds, unsigned voff, unsigned soff, u64 mask) {
  if constexpr (AUX == 0)
    asm volatile("s_mov_b32 m0, %0\n\ts_mov_b64 exec, %4\n\tbuffer_load_dwordx4 %1, %2, %3 offen lds\n\ts_mov_b64 exec, -1"
                 ::"s"(lds), "v"(voff), "s"(srd), "s"(soff), "s"(mask) : "memory");
  else
    asm volatile("s_mov_b32 m0, %0\n\ts_mov_b64 exec, %4\n\tbuffer_load_dwordx4 %1, %2, %3 offen sc1 lds\n\ts_mov_b64 exec, -1"
                 ::"s"(lds), "v"(voff), "s"(srd), "s"(soff), "s"(mask) : "memory");
}
DEVI void dma2(u32x4 srd, unsigned lds, unsigned v0, unsigned v1, unsigned soff) {
  asm volatile("s_mov_b32 m0, %0\n\ts_nop 0\n\tbuffer_load_dwordx4 %1, %3, %4 offen lds\n\tbuffer_load_dwordx4 %2, %3, %4 offen offset:1024 lds"
               ::"s"(lds), "v"(v0), "v"(v1), "s"(srd), "s"(soff) : "memory");
}
DEVI void dma4(u32x4 srd, unsigned lds, const unsigned (&v)[4], unsigned soff) {
  asm volatile("s_mov_b32 m0, %0\n\ts_nop 0\n\tbuffer_load_dwordx4 %1, %5, %6 offen lds\n\tbuffer_load_dwordx4 %2, %5, %6 offen offset:1024 lds\n\t"
               "buffer_load_dwordx4 %3, %5, %6 offen offset:2048 lds\n\tbuffer_load_dwordx4 %4, %5, %6 offen offset:3072 lds"
               ::"s"(lds), "v"(v[0]), "v"(v[1]), "v"(v[2]), "v"(v[3]), "s"(srd), "s"(soff) : "memory");
}
DEVI void dma2h(u32x4 srd, unsigned lds, const unsigned (&v)[3], unsigned soff) {
  asm volatile("s_mov_b32 m0, %0\n\ts_nop 0\n\tbuffer_load_dwordx4 %1, %4, %5 offen lds\n\tbuffer_load_dwordx4 %2, %4, %5 offen offset:1024 lds\n\t"
               "s_mov_b32 exec_hi, 0\n\tbuffer_load_dwordx4 %3, %4, %5 offen offset:2048 lds\n\ts_mov_b32 exec_hi, -1"
               ::"s"(lds), "v"(v[0]), "v"(v[1]), "v"(v[2]), "s"(srd), "s"(soff) : "memory");
}
DEVI void dma3s(u32x4 srd, unsigned lds, const unsigned (&v)[3], unsigned soff, u64 m12) {
  asm volatile("s_mov_b32 m0, %0\n\ts_mov_b64 exec, %6\n\tbuffer_load_dwordx4 %2, %4, %5 offen offset:1024 sc1 lds\n\t"
               "buffer_load_dwordx4 %3, %4, %5 offen offset:2048 sc1 lds\n\ts_mov_b64 exec, -1\n\tbuffer_load_dwordx4 %1, %4, %5 offen sc1 lds"
               ::"s"(lds), "v"(v[0]), "v"(v[1]), "v"(v[2]), "s"(srd), "s"(soff), "s"(m12) : "memory");
}
DEVI void dma4s(u32x4 srd, unsigned lds, const unsigned (&v)[4], unsigned soff, u64 m0, u64 m3) {
  asm volatile("s_mov_b32 m0, %0\n\ts_mov_b64 exec, %7\n\tbuffer_load_dwordx4 %1, %5, %6 offen sc1 lds\n\t"
               "s_mov_b64 exec, %8\n\tbuffer_load_dwordx4 %4, %5, %6 offen offset:3072 sc1 lds\n\ts_mov_b64 exec, -1\n\t"
               "buffer_load_dwordx4 %2, %5, %6 offen offset:1024 sc1 lds\n\tbuffer_load_dwordx4 %3, %5, %6 offen offset:2048 sc1 lds"
               ::"s"(lds), "v"(v[0]), "v"(v[1]), "v"(v[2]), "v"(v[3]), "s"(srd), "s"(soff), "s"(m0), "s"(m3) : "memory");
}
DEVI void touch(u32x4 srd, unsigned lds, unsigned voff) {
  asm volatile("s_mov_b32 m0, %0\n\ts_nop 0\n\tbuffer_load_dword %1, %2, 0 offen lds" ::"s"(lds), "v"(voff), "s"(srd) : "memory");
}
template <int AUX> DEVI void st128(u32x4 v, rsrc_t r, unsigned voff, unsigned soff) {
  unsigned o = voff + soff;
  asm volatile("" : "+v"(o));
  __builtin_amdgcn_raw_buffer_store_b128(v, r, o, 0, AUX);
}
DEVI int lane_id() { int v = threadIdx.x & 63; asm volatile("" : "+v"(v)); return v; }
DEVI unsigned lds_addr(const char* p) { return (unsigned)(size_t)(const LAS char*)p; }
template <int N> DEVI void wait_vm() { asm volatile("s_waitcnt vmcnt(%0)" ::"i"(N) : "memory"); }
template <int N> DEVI void wait_bar() { asm volatile("s_waitcnt vmcnt(%0)\n\ts_barrier" ::"i"(N) : "memory"); }
DEVI void lds_bar() { asm volatile("s_waitcnt lgkmcnt(0)\n\ts_barrier" ::: "memory"); }
DEVI void bare_bar() { asm volatile("s_barrier" ::: "memory"); }
DEVI void wait_lds() { asm volatile("s_waitcnt lgkmcnt(0)" ::: "memory"); }
DEVI bf16x8 tr2(const char* a, const char* b) {
  s16x4 lo = __builtin_amdgcn_ds_read_tr16_b64_v4i16((LAS s16x4*)a);
  s16x4 hi = __builtin_amdgcn_ds_read_tr16_b64_v4i16((LAS s16x4*)b);
  return __builtin_bit_cast(bf16x8, __builtin_shufflevector(lo, hi, 0, 1, 2, 3, 4, 5, 6, 7));
}
DEVI bf16x8 lds_frag(const char* a) { return *(const LAS bf16x8*)a; }
DEVI bf16x8 tr2a(unsigned a, int o0, int o1) {
  const LAS char* p = (const LAS char*)(size_t)a;
  s16x4 lo = __builtin_amdgcn_ds_read_tr16_b64_v4i16((LAS s16x4*)(p + o0));
  s16x4 hi = __builtin_amdgcn_ds_read_tr16_b64_v4i16((LAS s16x4*)(p + o1));
  return __builtin_bit_cast(bf16x8, __builtin_shufflevector(lo, hi, 0, 1, 2, 3, 4, 5, 6, 7));
}
DEVI bf16x8 lds_fraga(unsigned a, int o) { return *(const LAS bf16x8*)((const LAS char*)(size_t)a + o); }
DEVI f32x4 mfma(bf16x8 a, bf16x8 b, f32x4 c) { return __builtin_amdgcn_mfma_f32_16x16x32_bf16(a, b, c, 0, 0, 0); }
DEVI unsigned pk(float a, float b) { f32x2 v = {a, b}; return __builtin_bit_cast(unsigned, __builtin_convertvector(v, bf16x2)); }
DEVI bf16x8 mk_frag(unsigned a, unsigned b, unsigned c, unsigned d) { u32x4 v = {a, b, c, d}; return __builtin_bit_cast(bf16x8, v); }
DEVI float lo_f(unsigned u) { return __uint_as_float(u << 16); }
DEVI float hi_f(unsigned u) { return __uint_as_float(u & 0xffff0000u); }
DEVI f32x2 lo2(f32x4 v) { return __builtin_shufflevector(v, v, 0, 1); }
DEVI f32x2 hi2(f32x4 v) { return __builtin_shufflevector(v, v, 2, 3); }
DEVI f32x2 sp2(float a) { return (f32x2){a, a}; }
DEVI f32x2 fma2(f32x2 a, f32x2 b, f32x2 c) { return __builtin_elementwise_fma(a, b, c); }
DEVI unsigned pk2(f32x2 v) { return __builtin_bit_cast(unsigned, __builtin_convertvector(v, bf16x2)); }
DEVI float max2(float a, float b) { return __builtin_elementwise_maximum(a, b); }
DEVI void swap32(float& a, float& b) { auto r = __builtin_amdgcn_permlane32_swap(__float_as_uint(a), __float_as_uint(b), false, false); a = __uint_as_float(r[0]); b = __uint_as_float(r[1]); }
DEVI void swap16(float& a, float& b) { auto r = __builtin_amdgcn_permlane16_swap(__float_as_uint(a), __float_as_uint(b), false, false); a = __uint_as_float(r[0]); b = __uint_as_float(r[1]); }
template <bool MAX> DEVI float red4(float a, float b, float c, float d) {
  swap32(a, b); swap32(c, d);
  float u = MAX ? max2(a, b) : a + b, v = MAX ? max2(c, d) : c + d;
  swap16(u, v);
  return MAX ? max2(u, v) : u + v;
}
DEVI void bcast4(float r, float& a, float& b, float& c, float& d) {
  float u = r, v = r;
  swap16(u, v);
  a = u; b = u; swap32(a, b);
  c = v; d = v; swap32(c, d);
}
template <int NM, int ND, int NV = 0> DEVI void weave() {
#pragma unroll
  for (int i = 0; i < NM; ++i) {
    __builtin_amdgcn_sched_group_barrier(0x008, 1, 0);
    __builtin_amdgcn_sched_group_barrier(0x100, ND, 0);
    if (NV) __builtin_amdgcn_sched_group_barrier(0x002, NV, 0);
  }
}
DEVI float dot2(unsigned a, unsigned b, float c) { return __builtin_amdgcn_fdot2_f32_bf16(__builtin_bit_cast(bf16x2, a), __builtin_bit_cast(bf16x2, b), c, false); }

constexpr unsigned SPIN_CAP = 1u << 17;
DEVI unsigned umin(unsigned a, unsigned b) { return a < b ? a : b; }
DEVI u32x4 lds_ld4(char* sm, int off) { return *(volatile LAS u32x4*)(sm + off); }
DEVI unsigned min4(u32x4 v) { return __builtin_amdgcn_readfirstlane(umin(umin(v[0], v[1]), umin(v[2], v[3]))); }
DEVI void lds_set(char* sm, int off, unsigned v) {
  asm volatile("s_mov_b64 exec, 1\n\tds_write_b32 %0, %1\n\ts_mov_b64 exec, -1" ::"v"(lds_addr(sm) + off), "v"(v) : "memory");
}
template <int NW> DEVI unsigned lds_wait(char* sm, int off, unsigned need) {
  unsigned m = 0;
#pragma unroll 1
  for (unsigned spin = 0; spin < SPIN_CAP; ++spin) {
    if (NW == 1) m = __builtin_amdgcn_readfirstlane(*(volatile LAS unsigned*)(sm + off));
    else if (NW == 4) m = min4(lds_ld4(sm, off));
    else if (NW == 5) { const u32x4 a = lds_ld4(sm, off); const unsigned b = *(volatile LAS unsigned*)(sm + off + 16); m = min4((u32x4){umin(a[0], b), a[1], a[2], a[3]}); }
    else { const u32x4 a = lds_ld4(sm, off), b = lds_ld4(sm, off + 16); m = min4((u32x4){umin(a[0], b[0]), umin(a[1], b[1]), umin(a[2], b[2]), umin(a[3], b[3])}); }
    if (m >= need) break;
    __builtin_amdgcn_s_sleep(1);
  }
  asm volatile("" ::: "memory");
  return m;
}
DEVI u64 next_tag(u64 e) { return e * 6364136223846793005ull + 1442695040888963407ull; }
DEVI u64 ld_agent(const char* p) { return __hip_atomic_load((const GAS u64*)p, __ATOMIC_RELAXED, __HIP_MEMORY_SCOPE_AGENT); }
DEVI void st_agent(char* p, u64 v) { __hip_atomic_store((GAS u64*)p, v, __ATOMIC_RELAXED, __HIP_MEMORY_SCOPE_AGENT); }
DEVI u64 get_tag(char* sm) { return next_tag(*(volatile LAS u64*)(sm + L_EP)); }
DEVI void poll_flags(const char* flag, u64 tag) {
#pragma unroll 1
  for (unsigned spin = 0; spin < SPIN_CAP; ++spin) {
    if (__all(ld_agent(flag) == tag)) break;
    __builtin_amdgcn_s_sleep(1);
  }
  asm volatile("" ::: "memory");
}
DEVI void publish(char* sm, int k, unsigned nwaves, char* flag) {
  wait_vm<0>();
  if ((threadIdx.x & 63) == 0) {
    const unsigned old = __hip_atomic_fetch_add((LAS unsigned*)(sm + L_PUB + k * 4), 1u, __ATOMIC_RELAXED, __HIP_MEMORY_SCOPE_WORKGROUP);
    if (old == nwaves - 1) st_agent(flag, get_tag(sm));
  }
}

DEVI void dual_exp(const float (&p)[2][5], const float sc, float (&e)[2][10], float& s) {
  float mx[2], mn[2];
#pragma unroll
  for (int h = 0; h < 2; ++h) {
    mx[h] = max2(max2(max2(max2(p[h][0], p[h][1]), p[h][2]), p[h][3]), p[h][4]);
    mn[h] = max2(max2(max2(max2(-p[h][0], -p[h][1]), -p[h][2]), -p[h][3]), -p[h][4]);
  }
  const float m = -sc * red4<true>(mx[0], mn[0], mx[1], mn[1]);
  float c[4];
  bcast4(m, c[0], c[1], c[2], c[3]);
  float sp[2], sn[2];
#pragma unroll
  for (int h = 0; h < 2; ++h) {
#pragma unroll
    for (int i = 0; i < 5; ++i) {
      e[h][i] = __builtin_amdgcn_exp2f(fmaf(p[h][i], sc, c[2 * h]));
      e[h][5 + i] = __builtin_amdgcn_exp2f(fmaf(p[h][i], -sc, c[2 * h + 1]));
    }
    sp[h] = (e[h][0] + e[h][1]) + (e[h][2] + e[h][3]) + e[h][4];
    sn[h] = (e[h][5] + e[h][6]) + (e[h][7] + e[h][8]) + e[h][9];
  }
  s = red4<false>(sp[0], sn[0], sp[1], sn[1]);
}

template <int I, int N, class F> DEVI void static_for(F&& f) {
  if constexpr (I < N) { f(std::integral_constant<int, I>{}); static_for<I + 1, N>(f); }
}
constexpr int SSTEP = 2;
constexpr int c_tail(int i) { return i < 1 || i > 16 ? 0 : 2 + (i - 1 < NLOOP ? 2 : 0) + (i == SSTEP ? 10 : 0); }
constexpr int c_full(int i) { return i > 16 ? 0 : 4 + c_tail(i); }
constexpr int c_younger(int s) { return c_tail(s - 3) + c_full(s - 2) + c_full(s - 1); }

struct Loader {
  const Params& P; char* sm; const Tiles& T; int lw, l; unsigned sb;
  u32x4 rws;
  u32x4 sW;
  unsigned v0, v1, off, step, dst;
  int left, seg;

  DEVI void next_seg() {
    const int h = lw >> 1, hj = lw & 1;
    ++seg;
    if (seg < 2 && !T.hasB) seg = 2;
    if (seg == 2 && !T.hasC) seg = 3;
    off = 0; left = 0;
    const int gg = l >> 5, e = (l >> 3) & 3;
    const unsigned sw = ((l & 7) ^ (((l >> 4) & 3) << 1)) * 16;
    if (seg == 0) { sW = mk_srd(P.wo, D * D * 2); v0 = (40 * h + 20 * hj + 4 * gg + e) * 1280 + T.ctB * 128 + sw; v1 = v0 + 8 * 1280 - 1024; step = 80 * 1280; left = 8; }
    else if (seg == 1) { v0 = (160 * h + 40 * gg + 20 * hj + 16 + e) * 1280 + T.ctB * 128 + sw; v1 = v0 + 80 * 1280 - 1024; step = 320 * 1280; left = 2; }
    else if (seg == 2) { sW = mk_srd(h ? P.wu : P.wg, D * DF * 2); v0 = (4 * gg + 8 * hj + e) * 3456 + T.ctC * 128 + sw; v1 = v0 + 16 * 3456 - 1024; step = 32 * 3456; left = 20; }
    else if (seg == 3) { sW = mk_srd(P.wd, DF * D * 2); v0 = (16 * lw + (l >> 3)) * 1280 + T.ctB * 128 + sw; v1 = v0 + 8 * 1280 - 1024; step = 64 * 1280; left = WR_N; }
  }
  DEVI void issue_w() {
    if (left == 0) return;
    dma2(sW, dst, v0, v1, off);
    off += step;
    dst = dst + 8192 >= sb + MISC ? dst - (WR_N - 1) * 8192 : dst + 8192;
    if (--left == 0) next_seg();
  }
  DEVI void issue_w_in_c(int s) {
    dma2(sW, dst, v0, v1, off);
    off += step;
    dst = dst + 8192 >= sb + MISC ? dst - (WR_N - 1) * 8192 : dst + 8192;
    --left;
    if (s == 9) next_seg();
  }
  DEVI void hop(const char* flag, unsigned id) {
    poll_flags(flag, get_tag(sm));
  }
  template <int R, int SLOT, int NA, class F> DEVI void head(F&& pieces) {
    pieces(0, sb + AR);
    pieces(1, sb + AR + SLOT);
    wait_bar<NA>();
#pragma unroll
    for (int s = 2; s < R; ++s) pieces(s, sb + AR + s * SLOT);
  }
};

DEVI void loader_main(const Params& P, char* sm, const Tiles& T, const int lw, Dbg& dbg) {
  const int l = threadIdx.x & 63;
  const int t = l & 15, g = l >> 4, q = (l & 15) >> 2, pp = l & 3;
  const int p = T.p, j = T.j;
  const unsigned sb = lds_addr(sm);
  const u32x4 rx = mk_srd(P.x, S * D * 2), rw = mk_srd(P.wqkv, D * 3 * D * 2);
  const int rot = ((2 * (j & 7) + (j >> 3) + 4 * (p & 3) + (p >> 2)) & 15) * 10 / 16 * ROTA;
  unsigned xoff[4], woff[3], qoff[3];
#pragma unroll
  for (int i = 0; i < 4; ++i) xoff[i] = (p * 128 + 32 * lw + 8 * i + (l >> 3)) * 1280 + (((l & 7) ^ ((4 * i + (l >> 4)) & 7)) * 16) - 1024 * i;
#pragma unroll
  for (int i = 0; i < 3; ++i) {
    const int e = i * 64 + l, sl = e / 10, ch = e % 10;
    const int r = 16 * lw + ((sl & 3) | ((sl & 4) << 1) | ((sl & 8) >> 1));
    woff[i] = r * 3840 + (ch < 5 ? 1280 + 16 * ch : 2560 + 16 * (ch - 5)) + 80 * j - 1024 * i;
  }
  int ks = rot;
  auto issue = [&](int s) {
    const unsigned st = sb + ((s + 2) & 3) * A_ST;
    dma2h(rw, st + 16384 + lw * 2560, woff, ks * 64 * 3840);
    dma4(rx, st + lw * 4096, xoff, ks * 128);
    ks = ks == 9 ? 0 : ks + 1;
  };
  __builtin_amdgcn_s_setprio(3);
  Loader L{P, sm, T, lw, l, sb};
  L.rws = mk_srd(P.ws, WS_END);
  issue(0);
  issue(1);
  const bf16x8 ones = mk_frag(0x3F803F80u, 0x3F803F80u, 0x3F803F80u, 0x3F803F80u);
  f32x4 csA = {0, 0, 0, 0}, csB = {0, 0, 0, 0};
  const int crd = 16384 + (16 * (g >> 1) + 4 * (g & 1) + q) * 160 + pp * 8;
  bf16x8 cf[3];
  auto col_rd = [&](int s) {
    const char* a = sm + ((s + 2) & 3) * A_ST + crd;
    const char* b = a + 128 + (lw & 1) * 5120;
    cf[0] = tr2(a + lw * 32, a + lw * 32 + 1280); cf[1] = tr2(a + lw * 32 + 5120, a + lw * 32 + 6400); cf[2] = tr2(b, b + 1280);
  };
  auto col_mm = [&](int s) {
    csA = mfma(cf[0], ones, csA);
    csA = mfma(cf[1], ones, csA);
    if ((s & 1) == (lw >> 1)) csB = mfma(cf[2], ones, csB);
    asm volatile("" : "+v"(csA), "+v"(csB), "+v"(cf[2]));
  };
#pragma unroll 1
  for (int s = 0; s < 8; ++s) {
    wait_bar<7>();
    col_rd(s);
    issue(s + 2);
    col_mm(s);
  }
#pragma unroll
  for (int i = 0; i < 3; ++i) {
    const int e = i * 64 + l, sl = e / 5, ch = e % 5, s16 = sl & 15;
    const int r = (sl & 16) | (s16 & 8) | ((s16 & 1) << 2) | ((s16 >> 1) & 3);
    qoff[i] = r * 3840 + 16 * ch + 80 * j - 1024 * i;
  }
  auto issue_q = [&](int gq) {
    const unsigned st = sb + A_WQ + gq * 10240 + lw * 2560;
    int r0 = gq * 128 + lw * 32 + rot * 64;
    if (r0 >= 640) r0 -= 640;
    dma2h(rw, st, qoff, r0 * 3840);
  };
  wait_bar<7>();
  col_rd(8);
  issue_q(0); issue_q(1);
  col_mm(8);
  wait_bar<6>();
  col_rd(9);
  col_mm(9);
  if (t == 0) {
    *(LAS f32x4*)(sm + L_CS + lw * 64 + g * 16) = csA;
    *(LAS f32x4*)(sm + L_PHI + lw * 64 + g * 16) = csB;
  }
  wait_lds();
  lds_set(sm, L_SIG + lw * 4, 1);
  issue_q(2); issue_q(3); issue_q(4);
  L.seg = -1; L.dst = sb + WR + lw * 2048;
  L.next_seg();
  {
    const unsigned ln = (blockIdx.x * 68 + (lw == 3 ? 64 + (l & 3) : l)) * 128;
    if (lw == 0) touch(mk_srd(P.wg, D * DF * 2), sb + L_JUNK, ln);
    else if (lw == 1) touch(mk_srd(P.wu, D * DF * 2), sb + L_JUNK, ln);
    else if (lw == 2) touch(mk_srd(P.wd, D * DF * 2), sb + L_JUNK, ln);
    else { touch(mk_srd(P.wg, D * DF * 2), sb + L_JUNK, ln); touch(mk_srd(P.wu, D * DF * 2), sb + L_JUNK, ln); touch(mk_srd(P.wd, D * DF * 2), sb + L_JUNK, ln); }
    if (lw == 3) wait_vm<3>(); else wait_vm<1>();
  }
  lds_set(sm, L_SIG + lw * 4, 2);
  lds_wait<8>(sm, L_BAR, 2);
  STAMPT(512, 1, 6);
  {
    const rsrc_t rws = mk_rsrc(P.ws, WS_END);
    const int hh = lw >> 1, db = lw & 1;
    const char* base = sm + A_TOK + (4 * g + q) * 288;
    const char* pa = base + hh * 96 + (pp ^ g) * 8;
    const int vg = db == 0 ? 5 * hh + pp : (pp == 0 ? 5 * hh + 4 : 10);
    const char* pb = base + 192 + (vg ^ g) * 8;
    f32x4 kv[3] = {{0, 0, 0, 0}, {0, 0, 0, 0}, {0, 0, 0, 0}};
#pragma unroll
    for (int k = 0; k < 4; ++k) {
      const bf16x8 vb = tr2(pb + k * 32 * 288, pb + (k * 32 + 16) * 288);
#pragma unroll
      for (int b = 0; b < 3; ++b) kv[b] = mfma(tr2(pa + k * 32 * 288 + b * 32, pa + (k * 32 + 16) * 288 + b * 32), vb, kv[b]);
    }
    const unsigned dst = WS_KVP + (j * 16 + p) * KVP_SZ;
    const int item = hh * 84 + (db == 0 ? l : 64 + g * 5 + t);
    if (db == 0 || t < 5) {
      const u32x4 m = {pk(kv[0][0], kv[0][1]), pk(kv[0][2], kv[0][3]), pk(kv[1][0], kv[1][1]), pk(kv[1][2], kv[1][3])};
      st128<SC1>(m, rws, item * 16, dst);
      __builtin_amdgcn_raw_buffer_store_b32(pk(kv[2][0], kv[2][1]), rws, 2688 + item * 4, dst, SC1);
    }
    STAMPT(512, 1, 7);
  }
  publish(sm, 0, 4, P.ws + WS_FKV + (j * 16 + p) * 128);
  STAMPT(512, 1, 4);
  lds_wait<4>(sm, L_SIG, 2);
  if (lw < 3) {
    const char* qrd = sm + A_WQ + (16 * (g >> 1) + 2 * (4 * (g & 1) + q)) * 80 + pp * 8 + lw * 32;
    f32x4 cq = {0, 0, 0, 0};
#pragma unroll
    for (int k = 0; k < 20; ++k) cq = mfma(tr2(qrd + k * 2560, qrd + k * 2560 + 80), ones, cq);
    if (t == 0) *(LAS f32x4*)(sm + L_CS + 320 + lw * 64 + g * 16) = cq;
  }
  wait_lds();
  lds_set(sm, L_SIG + lw * 4, 3);
  STAMPT(512, 1, 5);
#pragma unroll 1
  for (int i = 0; i < 4; ++i) L.issue_w();
  STAMPT(512, 1, 8);
  L.hop(P.ws + WS_FKV + (j * 16 + (l & 15)) * 128, 1);
  STAMPT(512, 1, 9);
  {
    const rsrc_t rws = mk_rsrc(P.ws, WS_END);
    const int item = lw * 64 + l;
    if (item < 210) {
      u32x4 part[16];
#pragma unroll
      for (int i = 0; i < 16; ++i) part[i] = __builtin_amdgcn_raw_buffer_load_b128(rws, item * 16, WS_KVP + (j * 16 + i) * KVP_SZ, SC1);
      float sum[8];
      unsigned sel_lo = 0x00003F80u, sel_hi = 0x3F800000u;
      asm volatile("" : "+v"(sel_lo), "+v"(sel_hi));
#pragma unroll
      for (int c = 0; c < 4; ++c) { sum[2 * c] = lo_f(part[0][c]); sum[2 * c + 1] = hi_f(part[0][c]); }
#pragma unroll
      for (int i = 1; i < 16; ++i)
#pragma unroll
        for (int c = 0; c < 4; ++c) { sum[2 * c] = dot2(part[i][c], sel_lo, sum[2 * c]); sum[2 * c + 1] = dot2(part[i][c], sel_hi, sum[2 * c + 1]); }
      *(LAS u32x4*)(sm + L_KVS + item * 16) = (u32x4){pk(sum[0], sum[1]), pk(sum[2], sum[3]), pk(sum[4], sum[5]), pk(sum[6], sum[7])};
    }
    wait_lds();
    lds_set(sm, L_SIG + lw * 4, 4);
  }
  STAMPT(512, 1, 10);
#pragma unroll 1
  for (int i = 0; i < 7; ++i) L.issue_w();
  lds_wait<8>(sm, L_BAR, 4);
  const bool short3 = lw == 3 && T.nbB == 5;
  const u64 mask3 = short3 ? 1ull : ~0ull;
  auto mk_a3 = [&](unsigned (&v)[3], unsigned base, int nkc) {
#pragma unroll
    for (int n = 0; n < 3; ++n) {
      const int a = 3 * lw + (short3 ? 0 : n);
      v[n] = base + ((T.tbB + (a >> 1)) * nkc + (a & 1)) * 1024 + l * 16 - 1024 * n;
    }
  };
  if (T.hasB) {
#pragma unroll
    for (int n = 0; n < 3; ++n)
      dma16(rx, sb + AR + 4 * 12288 + (lw + 4 * n) * 1024, (l >> 3) * 1280 + (((l & 7) ^ (l >> 3)) * 16), (T.tbB * 16 + (lw + 4 * n) * 8) * 1280 + T.ctB * 128);
    wait_bar<0>();
    bare_bar();
#pragma unroll 1
    for (int i = 0; i < 5; ++i) L.issue_w();
    const int p0 = T.tbB >> 3, p1 = (T.tbB + T.nbB - 1) >> 3;
    unsigned av[3];
    mk_a3(av, WS_O, 20);
    asm volatile("" : "+v"(av[0]), "+v"(av[1]), "+v"(av[2]));
    STAMPT(512, 2, 5);
    L.hop(P.ws + WS_FO + (((l & 16) ? p1 : p0) * 16 + (l & 15)) * 128, 2);
    STAMPT(512, 1, 11);
    if (blockIdx.x == 0 && lw == 0 && l == 0) st_agent(P.ws + WS_EPOCH, get_tag(sm));
    auto pieces = [&](int s, unsigned st) { dma3s(L.rws, st + lw * 3072, av, s * 2048, mask3); };
    L.head<4, 12288, 3>(pieces);
#pragma unroll 1
    for (int s = 1; s < 7; ++s) {
      wait_bar<6>();
      pieces(s + 3, sb + AR + (s == 1 ? 4 : s - 2) * 12288);
    }
    wait_bar<6>();
    wait_bar<3>();
    wait_bar<0>();
#pragma unroll 1
    for (int i = 0; i < 2; ++i) L.issue_w();
    bare_bar();
    bare_bar();
#pragma unroll 1
    for (int i = 0; i < 3; ++i) L.issue_w();
  }
  u32x4 wreg[32];
  const rsrc_t rd = mk_rsrc(P.wd, DF * D * 2);
  const unsigned vo = (16 * lw + (l >> 3)) * 1280 + T.ctB * 128 + ((l & 7) ^ (((l >> 4) & 3) << 1)) * 16;
  auto load_reg = [&](int i) {
    wreg[2 * i] = __builtin_amdgcn_raw_buffer_load_b128(rd, vo, (WR_N + i) * 64 * 1280, 0);
    wreg[2 * i + 1] = __builtin_amdgcn_raw_buffer_load_b128(rd, vo, ((WR_N + i) * 64 + 8) * 1280, 0);
  };
  if (T.hasC) {
    const int r0 = ((T.tbC + 1) * 25 - 1) / 128, r1 = ((T.tbC + T.nbC) * 25 - 1) / 128;
    const int fr = r0 + l / 10;
    f32x4 cg = {0, 0, 0, 0}, cu = {0, 0, 0, 0};
    int cslot = T.hasB ? 10 : 0;
    const int cw = WR + (4 * g + q) * 128 + pp * 8 + ((lw ^ ((2 * g + (q >> 1)) & 3)) * 32);
    auto cs_rd = [&]() {
      const char* a = sm + cw + cslot * 8192;
      cf[0] = tr2(a, a + 2048); cf[1] = tr2(a + 4096, a + 6144);
      cslot = cslot + 1 == WR_N ? 0 : cslot + 1;
    };
    auto cs_mm = [&]() {
      cg = mfma(cf[0], ones, cg);
      cu = mfma(cf[1], ones, cu);
      asm volatile("" : "+v"(cg), "+v"(cu));
    };
    auto csum = [&]() { cs_rd(); cs_mm(); };
    if (T.hasB) {
#pragma unroll 1
      for (int i = 0; i < 6; ++i) csum();
    }
    const int bb = lw == 3 ? 11 : 4 * lw;
    const bool no14 = lw == 3 && T.nbC == 14;
    const u64 mk0 = lw == 3 ? 1ull : ~0ull, mk3 = no14 ? 1ull : ~0ull;
    unsigned av[4];
#pragma unroll
    for (int n = 0; n < 4; ++n) av[n] = WS_H + (T.tbC + bb + (n == 3 && no14 ? 2 : n)) * 20 * 1024 + l * 16 - 1024 * n;
    asm volatile("" : "+v"(av[0]), "+v"(av[1]), "+v"(av[2]), "+v"(av[3]));
    STAMPT(512, 2, 8);
    L.hop(P.ws + WS_FH + ((fr < r1 ? fr : r1) * 10 + l % 10) * 128, 3);
    STAMPT(512, 2, 9);
    auto pieces = [&](int s, unsigned st) { dma4s(L.rws, st + bb * 1024, av, s * 1024, mk0, mk3); };
    L.head<4, 15360, 4>(pieces);
    if (!T.hasB) csum();
    const int tok = lw * 64 + l, tokc = tok < T.nbC * 16 ? tok : 0;
    f32x2 sp[10];
    static_for<1, 20>([&](auto sc) {
      constexpr int s = decltype(sc)::value;
      wait_bar<c_younger(s)>();
      if (s < 18 && (s >= 6 || !T.hasB)) cs_rd();
      if constexpr (s < 17) {
        pieces(s + 3, sb + AR + ((s - 1) & 3) * 15360);
        L.issue_w_in_c(s);
        if constexpr (s - 1 < NLOOP) load_reg(s - 1);
      }
      if constexpr (s == SSTEP) {
#pragma unroll
        for (int c = 0; c < 10; ++c)
          asm volatile("buffer_load_dwordx2 %0, %1, %2, %3 offen sc1" : "=v"(sp[c]) : "v"(tokc * 8), "s"(L.rws), "s"(WS_ST + (c * 2048 + T.tbC * 16) * 8) : "memory");
      }
      if constexpr (s == SSTEP + 4) {
        asm volatile("" : "+v"(sp[0]), "+v"(sp[1]), "+v"(sp[2]), "+v"(sp[3]), "+v"(sp[4]), "+v"(sp[5]), "+v"(sp[6]), "+v"(sp[7]), "+v"(sp[8]), "+v"(sp[9]));
        float s1 = 0.f, s2 = 0.f;
#pragma unroll
        for (int c = 0; c < 10; ++c) { s1 += sp[c][0]; s2 += sp[c][1]; }
        const float mean = s1 * (1.f / D);
        const float var = max2(s2 * (1.f / D) - mean * mean, 0.f) + 1e-5f;
        const float rstd = __builtin_amdgcn_rsqf(var);
        constexpr float C0 = -2.f * 0.7978845608028654f * 1.4426950408889634f, C1 = C0 * 0.044715f;
        *(LAS f32x4*)(sm + L_STAT + tok * 16) = (f32x4){-mean, C0 * rstd, C1 * rstd * rstd * rstd, var};
      }
      if (s < 18 && (s >= 6 || !T.hasB)) cs_mm();
      if constexpr (s == 17) {
        csum(); csum();
        if (t == 0) {
          *(LAS f32x4*)(sm + L_CS + lw * 128 + g * 16) = cg;
          *(LAS f32x4*)(sm + L_CS + lw * 128 + 64 + g * 16) = cu;
        }
      }
    });
    lds_bar();
#pragma unroll 1
    for (int i = 0; i < 4; ++i) L.issue_w();
#pragma unroll
    for (int i = NLOOP; i < 16; ++i) load_reg(i);
  } else {
#pragma unroll
    for (int i = 0; i < 16; ++i) load_reg(i);
  }
  if (T.hasB) {
    const int r0 = ((T.tbB + 1) * 9 - 1) / 128, r1 = ((T.tbB + T.nbB) * 9 - 1) / 128;
#pragma unroll
    for (int u = 0; u < 32; u += 8)
      asm volatile("" : "+v"(wreg[u]), "+v"(wreg[u + 1]), "+v"(wreg[u + 2]), "+v"(wreg[u + 3]), "+v"(wreg[u + 4]), "+v"(wreg[u + 5]), "+v"(wreg[u + 6]), "+v"(wreg[u + 7]));
    wait_bar<0>();
    bare_bar();
#pragma unroll
    for (int u = 0; u < 2 * WR_N; ++u) *(LAS u32x4*)(sm + WR + lw * 2048 + l * 16 + (u >> 1) * 8192 + (u & 1) * 1024) = wreg[u];
    unsigned av[3];
    mk_a3(av, WS_ACT, 54);
    asm volatile("" : "+v"(av[0]), "+v"(av[1]), "+v"(av[2]));
    STAMPT(512, 3, 8);
    L.hop(P.ws + WS_FA + ((l < 27 ? r0 : r1) * 27 + l % 27) * 128, 4);
    STAMPT(512, 3, 9);
    auto pieces = [&](int s, unsigned st) { dma3s(L.rws, st + lw * 3072, av, s * 2048, mask3); };
    wait_lds();
    L.head<4, 12288, 3>(pieces);
    unsigned st = sb + AR + 4 * 12288;
#pragma unroll 1
    for (int s = 1; s < 13; ++s) {
      wait_bar<6>();
      pieces(s + 3, st);
      st = st == sb + AR + 4 * 12288 ? sb + AR : st + 12288;
    }
    STAMPT(512, 3, 5);
#pragma unroll
    for (int s = 13; s < 24; ++s) {
      if (s < 20) wait_bar<6>();
      else asm volatile("s_waitcnt vmcnt(6) lgkmcnt(0)\n\ts_barrier" ::: "memory");
      pieces(s + 3, sb + AR + ((s + 3) % 5) * 12288);
      if (s < 18) {
        *(LAS u32x4*)(sm + WR + lw * 2048 + l * 16 + (s - 13) * 8192) = wreg[2 * s - 4];
        *(LAS u32x4*)(sm + WR + lw * 2048 + l * 16 + (s - 13) * 8192 + 1024) = wreg[2 * s - 3];
      }
    }
    STAMPT(512, 3, 7);
    wait_bar<6>();
    wait_bar<3>();
    wait_bar<0>();
  }
}

DEVI void a_comp(const Params& P, char* sm, const Tiles& T, const int w, Dbg& dbg) {
  const int p = T.p, j = T.j;
  int l = threadIdx.x & 63;
  int t = l & 15, g = l >> 4, q = (l & 15) >> 2, pp = l & 3;
  const rsrc_t rws = mk_rsrc(P.ws, WS_END);
  const bf16x8 ones = mk_frag(0x3F803F80u, 0x3F803F80u, 0x3F803F80u, 0x3F803F80u);
  int bx0 = 2 * A_ST + w * 2048 + t * 128 + ((g ^ ((t >> 1) & 7)) * 16), bx1 = bx0 ^ 64;
  int bw = 2 * A_ST + 16384 + (16 * (g >> 1) + 4 * (g & 1) + q) * 160 + pp * 8;
  bf16x8 xf[20];
  f32x4 acc[5], accS = {0, 0, 0, 0}, accG = {0, 0, 0, 0};
#pragma unroll
  for (int b = 0; b < 5; ++b) acc[b] = (f32x4){0, 0, 0, 0};
  bf16x8 wf[6];
  auto rdw = [&](int kc, int b) {
    const char* a = sm + ((kc >> 1) & 1) * A_ST + (kc & 1) * 5120 + bw + b * 32;
    return tr2(a, a + 1280);
  };
  auto rdx = [&](int kc) { xf[kc] = lds_frag(sm + ((kc >> 1) & 1) * A_ST + ((kc & 1) ? bx1 : bx0)); };
  STAMP(0, 0);
  if (w < 4) {
    const int hh = w >> 1, ob = w & 1;
    const char* raw = sm + L_KVS + w * 800;
    dma16m(mk_srd(P.wphi, 32 * 400 * 2), lds_addr(raw), l * 16, (2 * j + hh) * 800, (1ull << 50) - 1);
    wait_vm<0>();
    unsigned v[4];
#pragma unroll
    for (int jj = 0; jj < 8; ++jj) {
      const int d = 16 * (hh + (jj >> 2)) + 4 * g + (jj & 3) - 20 * hh;
      const int f = ob == 0 ? t : 16 + (t >> 2);
      const bool ok = d >= 0 && d < 20 && (ob == 0 || (t & 3) == 0);
      const unsigned e = *(const LAS u16*)(raw + (ok ? d * 40 + f * 2 : 0));
      const unsigned m = ok ? e : 0u;
      if (jj & 1) v[jj >> 1] |= m << 16; else v[jj >> 1] = m;
    }
    *(LAS u32x4*)(sm + L_PHF + w * 1024 + l * 16) = (u32x4){v[0], v[1], v[2], v[3]};
  } else if (w == 4) {
    dma16m<SC1>(mk_srd(P.ws, WS_END), lds_addr(sm) + L_EP, l * 16, WS_EPOCH, 1ull);
    wait_vm<0>();
  }
  lds_bar();
  STAMP(0, 1);
  rdx(0);
#pragma unroll
  for (int b = 0; b < 5; ++b) wf[b] = rdw(0, b);
#pragma unroll
  for (int kc = 0; kc < 17; ++kc) {
    if (kc & 1) { bare_bar(); if (kc == 3) STAMP(0, 3); if (kc == 9) STAMP(0, 6); }
    if ((kc & 3) == 3) {
      const int d = (kc & 4) ? 2 * A_ST : -2 * A_ST;
      bx0 += d; bx1 += d; bw += d;
      asm volatile("" : "+v"(bx0), "+v"(bx1), "+v"(bw));
    }
    rdx(kc + 1);
#pragma unroll
    for (int b = 0; b < 5; ++b) {
      acc[b] = mfma(wf[b], xf[kc], acc[b]);
      wf[b] = rdw(kc + 1, b);
      if (b == 1) { accG = mfma(xf[kc], xf[kc], accG); asm volatile("" : "+v"(accG)); }
      if (b == 3) { accS = mfma(ones, xf[kc], accS); asm volatile("" : "+v"(accS)); }
      __builtin_amdgcn_sched_barrier(0);
    }
  }
  accG = mfma(xf[17], xf[17], accG);
  accS = mfma(ones, xf[17], accS);
  asm volatile("" : "+v"(accG), "+v"(accS));
  __builtin_amdgcn_sched_barrier(0);
  bare_bar();
  STAMP(0, 10);
  __builtin_amdgcn_s_setprio(2);
  rdx(18); rdx(19);
#pragma unroll
  for (int b = 0; b < 5; ++b) { acc[b] = mfma(wf[b], xf[17], acc[b]); wf[b] = rdw(18, b); __builtin_amdgcn_sched_barrier(0); }
  accG = mfma(xf[18], xf[18], accG); accG = mfma(xf[19], xf[19], accG);
  accS = mfma(ones, xf[18], accS); accS = mfma(ones, xf[19], accS);
  asm volatile("" : "+v"(accG), "+v"(accS));
  __builtin_amdgcn_sched_barrier(0);
#pragma unroll
  for (int b = 0; b < 5; ++b) { acc[b] = mfma(wf[b], xf[18], acc[b]); wf[b] = rdw(19, b); __builtin_amdgcn_sched_barrier(0); }
  l = lane_id(); t = l & 15; g = l >> 4; q = (l & 15) >> 2; pp = l & 3;
  float dg = (t & 2) ? ((t & 1) ? accG[3] : accG[2]) : ((t & 1) ? accG[1] : accG[0]);
  dg = __uint_as_float(__builtin_amdgcn_ds_bpermute(((t >> 2) * 16 + t) * 4, __float_as_uint(dg)));
  lds_wait<4>(sm, L_SIG, 1);
#pragma unroll
  for (int b = 0; b < 5; ++b) acc[b] = mfma(wf[b], xf[19], acc[b]);
  STAMP(0, 11);
  const float mean = accS[0] * (1.f / D);
  const float rstd = __builtin_amdgcn_rsqf(max2(dg * (1.f / D) - mean * mean, 0.f) + 1e-5f);
  const float sc = rstd * 1.4426950408889634f;

  {
    float v[5][4];
#pragma unroll
    for (int b = 0; b < 5; ++b) {
      f32x4 cs;
      if (b < 4) cs = *(const LAS f32x4*)(sm + L_CS + b * 64 + g * 16);
      else cs = (*(const LAS f32x4*)(sm + L_PHI + g * 16) + *(const LAS f32x4*)(sm + L_PHI + 64 + g * 16)) +
                (*(const LAS f32x4*)(sm + L_PHI + 128 + g * 16) + *(const LAS f32x4*)(sm + L_PHI + 192 + g * 16));
#pragma unroll
      for (int r = 0; r < 4; ++r) v[b][r] = fmaf(-mean, cs[r], acc[b][r]);
    }
    unsigned kd[3][2];
#pragma unroll
    for (int b = 0; b < 3; ++b) { kd[b][0] = pk(v[b][0], v[b][1]); kd[b][1] = pk(v[b][2], v[b][3]); }
    float pa[2][5];
#pragma unroll
    for (int hh = 0; hh < 2; ++hh) {
      const bf16x8 kb = mk_frag(kd[hh][0], kd[hh][1], kd[hh + 1][0], kd[hh + 1][1]);
      const f32x4 z = {0, 0, 0, 0};
      const f32x4 p0 = mfma(lds_frag(sm + L_PHF + hh * 2048 + l * 16), kb, z), p1 = mfma(lds_frag(sm + L_PHF + hh * 2048 + 1024 + l * 16), kb, z);
      pa[hh][0] = p0[0]; pa[hh][1] = p0[1]; pa[hh][2] = p0[2]; pa[hh][3] = p0[3]; pa[hh][4] = p1[0];
    }
    asm volatile("" : "+v"(pa[0][0]), "+v"(pa[0][4]), "+v"(pa[1][0]), "+v"(pa[1][4]));
    STAMP(0, 9);
    char* row = sm + A_TOK + (16 * w + t) * 288;
    const int gs = (g ^ q) * 8, gs2 = ((g ^ 2) ^ q) * 8;
    {
      const u32x2 v2 = {pk(v[2][0] * rstd, v[2][1] * rstd), pk(v[2][2] * rstd, v[2][3] * rstd)};
      const u32x2 one = {0x3F80u, 0u};
      const int hi = g >= 2 ? 32 : 0;
      *(LAS u32x2*)(row + (g >= 2 ? 192 : 256) + gs2) = g >= 2 ? v2 : one;
      *(LAS u32x2*)(row + 192 + hi + gs2) = (u32x2){pk(v[3][0] * rstd, v[3][1] * rstd), pk(v[3][2] * rstd, v[3][3] * rstd)};
      *(LAS u32x2*)(row + 224 + hi + gs2) = (u32x2){pk(v[4][0] * rstd, v[4][1] * rstd), pk(v[4][2] * rstd, v[4][3] * rstd)};
    }
    float e[2][10], s, r[4];
    dual_exp(pa, sc, e, s);
    asm volatile("" : "+v"(s));
    STAMP(0, 13);
    bcast4(__builtin_amdgcn_rcpf(s), r[0], r[1], r[2], r[3]);
#pragma unroll
    for (int hh = 0; hh < 2; ++hh) {
      const float rp = r[2 * hh], rn = r[2 * hh + 1];
      *(LAS u32x2*)(row + hh * 96 + gs) = (u32x2){pk(e[hh][0] * rp, e[hh][1] * rp), pk(e[hh][2] * rp, e[hh][3] * rp)};
      *(LAS u32x2*)(row + hh * 96 + 32 + gs) = (u32x2){pk(e[hh][4] * rp, e[hh][5] * rn), pk(e[hh][6] * rn, e[hh][7] * rn)};
      *(LAS u32x2*)(row + hh * 96 + 64 + gs) = (u32x2){pk(e[hh][8] * rn, e[hh][9] * rn), 0u};
    }
  }
  wait_lds();
  lds_set(sm, L_BAR + w * 4, 2);
  __builtin_amdgcn_s_setprio(0);
  STAMP(0, 12);

  f32x4 aq[3] = {{0, 0, 0, 0}, {0, 0, 0, 0}, {0, 0, 0, 0}};
  lds_wait<4>(sm, L_SIG, 2);
  {
    const char* qrd = sm + A_WQ + (16 * (g >> 1) + 2 * (4 * (g & 1) + q)) * 80 + pp * 8;
    auto rq = [&](int u) { const char* a = qrd + (u / 3) * 2560 + (u % 3) * 32; return tr2(a, a + 80); };
#pragma unroll
    for (int u = 0; u < 6; ++u) wf[u] = rq(u);
#pragma unroll
    for (int u = 0; u < 60; ++u) {
      aq[u % 3] = mfma(wf[u % 6], xf[u / 3], aq[u % 3]);
      if (u + 6 < 60) wf[u % 6] = rq(u + 6);
      __builtin_amdgcn_sched_barrier(0);
    }
  }
  lds_wait<4>(sm, L_SIG, 3);
  wait_lds();
  lds_set(sm, L_BAR + w * 4, 4);
  STAMP(0, 14);
  unsigned fq[2][5];
  float spq[2];
  {
    unsigned qd[3][2];
#pragma unroll
    for (int b = 0; b < 3; ++b) {
      const f32x4 cs = *(const LAS f32x4*)(sm + L_CS + 320 + b * 64 + g * 16);
      float v[4];
#pragma unroll
      for (int r = 0; r < 4; ++r) v[r] = fmaf(-mean, cs[r], aq[b][r]);
      qd[b][0] = pk(v[0], v[1]); qd[b][1] = pk(v[2], v[3]);
    }
    if (g >= 2) { qd[2][0] = 0u; qd[2][1] = 0u; }
    float pa[2][5];
#pragma unroll
    for (int hh = 0; hh < 2; ++hh) {
      const bf16x8 qb = mk_frag(qd[hh][0], qd[hh][1], qd[hh + 1][0], qd[hh + 1][1]);
      const f32x4 z = {0, 0, 0, 0};
      const f32x4 p0 = mfma(lds_frag(sm + L_PHF + hh * 2048 + l * 16), qb, z), p1 = mfma(lds_frag(sm + L_PHF + hh * 2048 + 1024 + l * 16), qb, z);
      pa[hh][0] = p0[0]; pa[hh][1] = p0[1]; pa[hh][2] = p0[2]; pa[hh][3] = p0[3]; pa[hh][4] = p1[0];
    }
    float e[2][10], s, sv4[4];
    dual_exp(pa, sc, e, s);
    bcast4(s, sv4[0], sv4[1], sv4[2], sv4[3]);
#pragma unroll
    for (int hh = 0; hh < 2; ++hh) {
      const float rn = sv4[2 * hh] * __builtin_amdgcn_rcpf(sv4[2 * hh + 1]);
      spq[hh] = sv4[2 * hh];
      fq[hh][0] = pk(e[hh][0], e[hh][1]); fq[hh][1] = pk(e[hh][2], e[hh][3]);
      fq[hh][2] = pk(e[hh][4], e[hh][5] * rn); fq[hh][3] = pk(e[hh][6] * rn, e[hh][7] * rn);
      fq[hh][4] = pk(e[hh][8] * rn, e[hh][9] * rn);
    }
    asm volatile("" : "+v"(fq[0][0]), "+v"(fq[0][1]), "+v"(fq[0][2]), "+v"(fq[0][3]), "+v"(fq[0][4]),
                      "+v"(fq[1][0]), "+v"(fq[1][1]), "+v"(fq[1][2]), "+v"(fq[1][3]), "+v"(fq[1][4]));
  }
  STAMP(0, 15);
  lds_wait<4>(sm, L_SIG, 4);
  STAMP(0, 16);

  {
    unsigned om[4], ol[4];
#pragma unroll
    for (int hh = 0; hh < 2; ++hh) {
      const bf16x8 b0 = mk_frag(fq[hh][0], fq[hh][1], fq[hh][2], fq[hh][3]);
      const bf16x8 b1 = mk_frag(fq[hh][4], 0u, 0u, 0u);
      f32x4 n[2];
#pragma unroll
      for (int db = 0; db < 2; ++db) {
        const int it = hh * 84 + (db == 0 ? l : 64 + g * 5 + (t < 5 ? t : 4));
        f32x4 c = {0, 0, 0, 0};
        c = mfma(lds_frag(sm + L_KVS + it * 16), b0, c);
        c = mfma(mk_frag(*(const LAS unsigned*)(sm + L_KVS + 2688 + it * 4), 0u, 0u, 0u), b1, c);
        n[db] = c;
      }
      float da = n[1][0], dbv = n[1][0];
      swap16(da, dbv);
      const float rd = __builtin_amdgcn_rcpf(fmaf(spq[hh], 1e-6f, dbv));
      om[2 * hh] = pk(n[0][0] * rd, n[0][1] * rd); om[2 * hh + 1] = pk(n[0][2] * rd, n[0][3] * rd);
      ol[2 * hh] = pk(n[1][0] * rd, n[1][1] * rd); ol[2 * hh + 1] = pk(n[1][2] * rd, n[1][3] * rd);
    }
    const unsigned tb = WS_O + (p * 8 + w) * 20 * 1024;
    st128<SC1>((u32x4){om[0], om[1], om[2], om[3]}, rws, l * 16, tb + j * 1024);
    if (g == 0) st128<SC1>((u32x4){ol[0], ol[1], ol[2], ol[3]}, rws, t * 16, tb + (16 + (j >> 2)) * 1024 + (j & 3) * 256);
  }
  STAMP(0, 17);
  publish(sm, 1, 8, P.ws + WS_FO + (p * 16 + j) * 128);
  STAMP(0, 18);
}

template <int NS, int NP>
DEVI void g_main(char* sm, int w, int ws0, bool three, f32x4 (&acc)[3]) {
  const int l = lane_id();
  const int g = l >> 4, q = (l & 15) >> 2, pp = l & 3, mh = w >> 2, cb = w & 3;
  const int wrd = WR + (4 * g + q) * 128 + pp * 8 + ((cb ^ ((2 * g + (q >> 1)) & 3)) * 32);
  bf16x8 wp[NP][2];
#pragma unroll
  for (int s = 0; s < NP; ++s) {
    const int slot = ws0 + s >= WR_N ? ws0 + s - WR_N : ws0 + s;
    const char* p = sm + wrd + slot * 8192;
    wp[s][0] = tr2(p, p + 2048);
    wp[s][1] = tr2(p + 4096, p + 6144);
  }
  lds_bar();
  const char* ard = sm + AR + mh * 6144 + l * 16;
  const char* wlo = sm + wrd;
  const char* whi = wlo + 6 * 8192;
  auto run = [&](auto nbc) {
    constexpr int NB = decltype(nbc)::value;
    bf16x8 af[2 * NB], wn[2][2];
    auto rda = [&](int s, int u) { return lds_frag(ard + (s % 5) * 12288 + (u % NB) * 2048 + (u / NB) * 1024); };
    auto rdw = [&](int s, int k) {
      const int slot = (s - NP) % WR_N;
      const char* p = (slot < 6 ? wlo + slot * 8192 : whi + (slot - 6) * 8192) + k * 4096;
      return tr2(p, p + 2048);
    };
    bare_bar();
#pragma unroll
    for (int u = 0; u < 2 * NB; ++u) af[u] = rda(0, u);
#pragma unroll
    for (int s = 0; s < NS; ++s) {
      if (s + 1 < NS) bare_bar();
#pragma unroll
      for (int u = 0; u < 2 * NB; ++u) {
        acc[u % NB] = mfma(s < NP ? wp[s < NP ? s : 0][u / NB] : wn[s & 1][u / NB], af[u], acc[u % NB]);
        if (s + 1 < NS) {
          if (s + 1 >= NP && u % NB == 0) wn[(s + 1) & 1][u / NB] = rdw(s + 1, u / NB);
          af[u] = rda(s + 1, u);
        }
        __builtin_amdgcn_sched_barrier(0);
      }
    }
  };
  if (three) run(std::integral_constant<int, 3>{}); else run(std::integral_constant<int, 2>{});
}

DEVI void b_comp(const Params& P, char* sm, const Tiles& T, int w, unsigned (&hp)[3][2], Dbg& dbg) {
  const int l = lane_id(), t = l & 15, g = l >> 4, mh = w >> 2, cb = w & 3;
  const rsrc_t rws = mk_rsrc(P.ws, WS_END);
  f32x4 acc[3];
  bare_bar();
#pragma unroll
  for (int m = 0; m < 3; ++m) {
    const u32x2 xr = *(const LAS u32x2*)(sm + AR + 4 * 12288 + (3 * mh + m) * 2048 + t * 128 + (((2 * cb + (g >> 1)) ^ (t & 7)) * 16) + (g & 1) * 8);
    acc[m] = (f32x4){lo_f(xr[0]), hi_f(xr[0]), lo_f(xr[1]), hi_f(xr[1])};
  }
  STAMP(1, 0);
  g_main<10, 10>(sm, w, 0, 3 * mh + 2 < T.nbB, acc);
  STAMP(1, 1);
#pragma unroll
  for (int m = 0; m < 3; ++m) { hp[m][0] = pk(acc[m][0], acc[m][1]); hp[m][1] = pk(acc[m][2], acc[m][3]); }
  {
    const unsigned hbase = WS_H + ((T.tbB + 3 * mh) * 20 + 2 * T.ctB + (cb >> 1)) * 1024 + (cb & 1) * 512;
    auto r0 = __builtin_amdgcn_permlane32_swap(hp[0][0], hp[1][0], false, false);
    auto r1 = __builtin_amdgcn_permlane32_swap(hp[0][1], hp[1][1], false, false);
    st128<SC1>((u32x4){r0[0], r1[0], r0[1], r1[1]}, rws, (l & 31) * 16 + (l >> 5) * 20 * 1024, hbase);
    auto r2 = __builtin_amdgcn_permlane32_swap(hp[2][0], hp[2][0], false, false);
    auto r3 = __builtin_amdgcn_permlane32_swap(hp[2][1], hp[2][1], false, false);
    if (l < 32 && 3 * mh + 2 < T.nbB) st128<SC1>((u32x4){r2[0], r3[0], r2[1], r3[1]}, rws, l * 16, hbase + 2 * 20 * 1024);
  }
  float st[3][2];
#pragma unroll
  for (int m = 0; m < 3; ++m) {
    st[m][0] = dot2(hp[m][1], 0x3F803F80u, dot2(hp[m][0], 0x3F803F80u, 0.f));
    st[m][1] = dot2(hp[m][1], hp[m][1], dot2(hp[m][0], hp[m][0], 0.f));
  }
  const float r01 = red4<false>(st[0][0], st[0][1], st[1][0], st[1][1]);
  swap32(st[2][0], st[2][1]);
  float u = st[2][0] + st[2][1], v = u;
  swap16(u, v);
  char* ex = sm + L_PHF + mh * 2048;
  *(LAS f32x2*)(ex + cb * 512 + l * 8) = (f32x2){r01, u + v};
  lds_bar();
  if (cb == 0) {
    const f32x2 o = (*(const LAS f32x2*)(ex + l * 8) + *(const LAS f32x2*)(ex + 512 + l * 8)) + (*(const LAS f32x2*)(ex + 1024 + l * 8) + *(const LAS f32x2*)(ex + 1536 + l * 8));
    const unsigned base = WS_ST + (T.ctB * 2048 + (T.tbB + 3 * mh) * 16) * 8;
    __builtin_amdgcn_raw_buffer_store_b32(__float_as_uint(o[0]), rws, ((g & 1) * 16 + t) * 8 + (g >> 1) * 4, base, SC1);
    if ((g & 1) == 0 && 3 * mh + 2 < T.nbB) __builtin_amdgcn_raw_buffer_store_b32(__float_as_uint(o[1]), rws, (32 + t) * 8 + (g >> 1) * 4, base, SC1);
  }
  STAMP(1, 2);
  publish(sm, 2, 8, P.ws + WS_FH + (T.rtB * 10 + T.ctB) * 128);
  STAMP(1, 3);
  bare_bar();
}

DEVI void d_comp(const Params& P, char* sm, const Tiles& T, int w, const unsigned (&hp)[3][2], Dbg& dbg) {
  f32x4 acc[3];
#pragma unroll
  for (int m = 0; m < 3; ++m) acc[m] = (f32x4){lo_f(hp[m][0]), hi_f(hp[m][0]), lo_f(hp[m][1]), hi_f(hp[m][1])};
  bare_bar();
  STAMP(3, 0);
  g_main<27, 11>(sm, w, (10 + (T.hasC ? 20 : 0)) % WR_N, 3 * (w >> 2) + 2 < T.nbB, acc);
  STAMP(3, 1);
  const int l = lane_id(), t = l & 15, g = l >> 4, mh = w >> 2, cb = w & 3;
  const rsrc_t ro = mk_rsrc(P.out, S * D * 2);
  char* stg = sm + WR + 6 * 8192;
#pragma unroll
  for (int m = 0; m < 3; ++m)
    *(LAS u32x2*)(stg + ((3 * mh + m) * 16 + t) * 144 + cb * 32 + g * 8) = (u32x2){pk(acc[m][0], acc[m][1]), pk(acc[m][2], acc[m][3])};
  lds_bar();
#pragma unroll
  for (int i = 0; i < 2; ++i) {
    const int row = (w + 8 * i) * 8 + (l >> 3);
    if (w + 8 * i < 2 * T.nbB) {
      const u32x4 v = *(const LAS u32x4*)(stg + row * 144 + (l & 7) * 16);
      st128<0>(v, ro, row * 1280 + (l & 7) * 16, T.tbB * 16 * 1280 + T.ctB * 128);
    }
  }
  STAMP(3, 2);
}

template <int NB, bool HALF>
DEVI void c_run(const Params& P, char* sm, const Tiles& T, int w, Dbg& dbg) {
  const int l = lane_id();
  const int t = l & 15, g = l >> 4, q = (l & 15) >> 2, pp = l & 3, mq = (w >> 1) & 1, nh = w & 1;
  const int b0 = NB == 4 ? 4 * mq : 8 + 3 * mq;
  const int wrow = WR + (4 * g + q) * 128 + pp * 8, wsw = (2 * g + (q >> 1)) & 3;
  const int c0 = 2 * nh + (HALF ? mq : 0), c1 = 2 * nh + (HALF ? 1 - mq : 1);
  int wslot = T.hasB ? 10 : 0, aslot = 0;
  const unsigned sb = lds_addr(sm);
  unsigned pa = sb + AR + b0 * 1024 + l * 16, pw0 = sb + wrow + ((c0 ^ wsw) * 32) + wslot * 8192, pw1 = sb + wrow + ((c1 ^ wsw) * 32) + wslot * 8192;
  unsigned ph = sb + AR + 14 * 1024 + l * 16;
  f32x4 ag[NB][2], au[NB][2], hg = {0, 0, 0, 0}, hu = {0, 0, 0, 0};
#pragma unroll
  for (int m = 0; m < NB; ++m)
#pragma unroll
    for (int c = 0; c < 2; ++c) { ag[m][c] = (f32x4){0, 0, 0, 0}; au[m][c] = (f32x4){0, 0, 0, 0}; }
  struct Frags { bf16x8 g[2], u[2], a[NB], h; };
  auto rd = [&](Frags& f) {
    f.g[0] = tr2a(pw0, 0, 2048);
    f.a[0] = lds_fraga(pa, 0);
    f.u[0] = tr2a(pw0, 4096, 6144);
    f.g[1] = tr2a(pw1, 0, 2048);
    f.u[1] = tr2a(pw1, 4096, 6144);
#pragma unroll
    for (int m = 1; m < NB; ++m) f.a[m] = lds_fraga(pa, m * 1024);
    if (HALF) f.h = lds_fraga(ph, 0);
  };
  auto adv = [&]() {
    const int da = aslot == 3 ? -3 * 15360 : 15360, dw = wslot == WR_N - 1 ? -(WR_N - 1) * 8192 : 8192;
    aslot = (aslot + 1) & 3;
    wslot = wslot == WR_N - 1 ? 0 : wslot + 1;
    pa += da; pw0 += dw; pw1 += dw;
    if (HALF) { ph += da; asm volatile("" : "+v"(ph)); }
    asm volatile("" : "+v"(pa), "+v"(pw0), "+v"(pw1));
  };
  auto mm = [&](const Frags& f) {
#pragma unroll
    for (int m = 0; m < NB; ++m)
#pragma unroll
      for (int c = 0; c < 2; ++c) { ag[m][c] = mfma(f.g[c], f.a[m], ag[m][c]); au[m][c] = mfma(f.u[c], f.a[m], au[m][c]); }
    if (HALF) { hg = mfma(f.g[0], f.h, hg); hu = mfma(f.u[0], f.h, hu); }
  };
  auto step = [&](const Frags& cur, Frags& nxt) {
    lds_bar();
    rd(nxt);
    mm(cur);
    if (NB == 4) weave<12, 1>();
    else { __builtin_amdgcn_sched_group_barrier(0x100, YPRE, 0); weave<(HALF ? 12 : 11) - YPRE, 1>(); }
    adv();
    __builtin_amdgcn_sched_barrier(0);
  };
  Frags f0, f1;
  lds_bar();
  STAMP(2, 1);
  rd(f0);
  adv();
#pragma unroll 1
  for (int s = 0; s + 2 < 20; s += 2) { step(f0, f1); step(f1, f0); }
  step(f0, f1);
  lds_bar();
  f32x4 csg[2], csu[2], rc[NB + 1];
  csg[0] = *(const LAS f32x4*)(sm + L_CS + c0 * 128 + g * 16); csu[0] = *(const LAS f32x4*)(sm + L_CS + c0 * 128 + 64 + g * 16);
  csg[1] = *(const LAS f32x4*)(sm + L_CS + c1 * 128 + g * 16); csu[1] = *(const LAS f32x4*)(sm + L_CS + c1 * 128 + 64 + g * 16);
#pragma unroll
  for (int m = 0; m < NB; ++m) rc[m] = *(const LAS f32x4*)(sm + L_STAT + ((b0 + m) * 16 + t) * 16);
  if (HALF) rc[NB] = *(const LAS f32x4*)(sm + L_STAT + (14 * 16 + t) * 16);
  mm(f1);
  STAMP(2, 2);
  const rsrc_t rws = mk_rsrc(P.ws, WS_END);
  auto glu2 = [&](f32x2 a, f32x2 b, f32x2 ca, f32x2 cb, f32x4 k) {
    const f32x2 G = fma2(sp2(k[0]), ca, a), U = fma2(sp2(k[0]), cb, b);
    const f32x2 y = G * fma2(G * G, sp2(k[2]), sp2(k[1]));
    const f32x2 e = {__builtin_amdgcn_exp2f(y[0]), __builtin_amdgcn_exp2f(y[1])};
    const f32x2 d = fma2(e, sp2(k[3]), sp2(k[3]));
    const f32x2 r = {__builtin_amdgcn_rcpf(d[0]), __builtin_amdgcn_rcpf(d[1])};
    return pk2(G * U * r);
  };
  const unsigned obase = WS_ACT + ((T.tbC + b0) * 54 + 2 * T.ctC + nh) * 1024;
#pragma unroll
  for (int m = 0; m < NB; ++m) {
    unsigned o[2][2];
#pragma unroll
    for (int c = 0; c < 2; ++c) {
      o[c][0] = glu2(lo2(ag[m][c]), lo2(au[m][c]), lo2(csg[c]), lo2(csu[c]), rc[m]);
      o[c][1] = glu2(hi2(ag[m][c]), hi2(au[m][c]), hi2(csg[c]), hi2(csu[c]), rc[m]);
    }
    if (HALF && mq) st128<SC1>((u32x4){o[1][0], o[1][1], o[0][0], o[0][1]}, rws, l * 16, obase + m * 54 * 1024);
    else st128<SC1>((u32x4){o[0][0], o[0][1], o[1][0], o[1][1]}, rws, l * 16, obase + m * 54 * 1024);
  }
  if (HALF) {
    const u32x2 o = {glu2(lo2(hg), lo2(hu), lo2(csg[0]), lo2(csu[0]), rc[NB]), glu2(hi2(hg), hi2(hu), hi2(csg[0]), hi2(csu[0]), rc[NB])};
    __builtin_amdgcn_raw_buffer_store_b64(o, rws, l * 16 + mq * 8, WS_ACT + ((T.tbC + 14) * 54 + 2 * T.ctC + nh) * 1024, SC1);
  }
}

DEVI void c_comp(const Params& P, char* sm, const Tiles& T, int w, Dbg& dbg) {
  STAMP(2, 0);
  if (w < 4) c_run<4, false>(P, sm, T, w, dbg);
  else if (T.nbC == 15) c_run<3, true>(P, sm, T, w, dbg);
  else c_run<3, false>(P, sm, T, w, dbg);
  STAMP(2, 3);
  publish(sm, 3, 8, P.ws + WS_FA + (T.rtC * 27 + T.ctC) * 128);
  STAMP(2, 4);
}

extern __shared__ __attribute__((aligned(16))) char smem[];

__global__ __launch_bounds__(NT) void k_block(Params P) {
  const int b = blockIdx.x, xcd = b & 7, idx = b >> 3;
  const int w = __builtin_amdgcn_readfirstlane(threadIdx.x >> 6);
  Tiles T;
  T.p = 4 * (xcd >> 1) + (idx >> 3); T.j = 8 * (xcd & 1) + (idx & 7);
  {
    const int qn = (xcd >> 1) * 32 + idx;
    T.hasB = qn < 125;
    T.rtB = qn / 5; T.ctB = 5 * (xcd & 1) + qn % 5;
    T.tbB = T.rtB * 128 / 25; T.nbB = (T.rtB + 1) * 128 / 25 - T.tbB;
  }
  {
    const int lo = xcd * 243 / 8, hi = (xcd + 1) * 243 / 8, qn = hi - 1 - idx;
    T.hasC = qn >= lo;
    const int qc = T.hasC ? qn : lo, band = qc / 81, rem = qc % 81;
    T.rtC = band * 3 + rem % 3; T.ctC = rem / 3;
    T.tbC = T.rtC * 128 / 9; T.nbC = (T.rtC + 1) * 128 / 9 - T.tbC;
  }
  Dbg dbg{};
  if (w >= 8) { loader_main(P, smem, T, w - 8, dbg); if (w == 8) DUMP(1); asm volatile(".Lkb_end1:"); return; }
  if (threadIdx.x < 32) *(LAS unsigned*)(smem + MISC + threadIdx.x * 4) = 0u;
  if (w == 7) {
    u64 pc; unsigned n;
    asm volatile("s_getpc_b64 %0\n.Lkb_here:\n\ts_mov_b32 %1, .Lkb_end1-.Lkb_here\n\ts_max_i32 %1, %1, .Lkb_end2-.Lkb_here\n\ts_max_i32 %1, %1, 0" : "=s"(pc), "=s"(n) :: "scc");
    const u32x4 rc = mk_srd((const void*)pc, n);
    touch(rc, lds_addr(smem) + L_JUNK, (idx * 16 + (threadIdx.x & 15)) * 128);
  }
  unsigned hp[3][2];
  a_comp(P, smem, T, w, dbg);
  if (T.hasB) b_comp(P, smem, T, w, hp, dbg);
  if (T.hasC) c_comp(P, smem, T, w, dbg);
  if (T.hasB) d_comp(P, smem, T, w, hp, dbg);
  if (w == DUMPW) DUMP(0);
  asm volatile(".Lkb_end2:");
}

extern "C" void kernel_launch(void* const* d_in, const int* in_sizes, int n_in, void* d_out, int out_size, void* d_ws, size_t ws_size,
                              hipStream_t stream) {
  Params P;
  P.x = (const u16*)d_in[0]; P.wqkv = (const u16*)d_in[1]; P.wphi = (const u16*)d_in[2]; P.wo = (const u16*)d_in[3];
  P.wg = (const u16*)d_in[5]; P.wu = (const u16*)d_in[6]; P.wd = (const u16*)d_in[7];
  P.out = (u16*)d_out; P.ws = (char*)d_ws;
  (void)hipFuncSetAttribute((const void*)k_block, hipFuncAttributeMaxDynamicSharedMemorySize, LDS_TOTAL);
  k_block<<<256, NT, LDS_TOTAL, stream>>>(P);
}
```
